```python
import torch

def _softmax(x, axis=-1):
    return torch.softmax(x, dim=axis)
def _gelu(x):
    return torch.nn.functional.gelu(x, approximate="tanh")
def _silu(x):
    return torch.nn.functional.silu(x)
def _sigmoid(x):
    return torch.sigmoid(x)
def _layernorm(x, axis=-1, eps=1e-5):
    m = x.mean(dim=axis, keepdim=True)
    v = x.var(dim=axis, keepdim=True, unbiased=False)
    return (x - m) / torch.sqrt(v + eps)
def _scatter_add(tgt, idx, src, axis=0):
    return tgt.clone().index_add_(axis, idx, src)
def _segment_sum(src, seg, n):
    out = torch.zeros((n,) + src.shape[1:], dtype=src.dtype)
    return out.index_add_(0, seg, src)
def _pad(x, pw):
    flat = tuple(v for pair in reversed(pw) for v in pair)
    return torch.nn.functional.pad(x, flat)

kernel_name = 'gated_mix__ssm_conv_s5__glu_gated_swiglu__prompt'

def setup_inputs():

    _w = (lambda a: (lambda t: t.to(torch.bfloat16) if t.is_floating_point() else t)(torch.as_tensor(a)))
    def _A_setup():
        B, S, D = 1, 2048, 1024
        P = D
        Lambda_re = -0.5 * (lambda _x: torch.exp(torch.as_tensor(_x)))((lambda *s: torch.randn(*s, dtype=torch.bfloat16) * 0.2)(P))
        Lambda_im = (lambda *s: torch.randn(*s, dtype=torch.bfloat16) * 0.4)(P) * 1.0
        B_re = (lambda *s: torch.randn(*s, dtype=torch.bfloat16) * 0.2)(P, D) * (D ** -0.5)
        B_im = (lambda *s: torch.randn(*s, dtype=torch.bfloat16) * 0.2)(P, D) * (D ** -0.5)
        C_re = (lambda *s: torch.randn(*s, dtype=torch.bfloat16) * 0.2)(D, P) * (P ** -0.5)
        C_im = (lambda *s: torch.randn(*s, dtype=torch.bfloat16) * 0.2)(D, P) * (P ** -0.5)
        log_dt = (lambda *s: torch.randn(*s, dtype=torch.bfloat16) * 0.2)(P) * 0.3 - 2.0
        Dskip = (lambda *s: torch.randn(*s, dtype=torch.bfloat16) * 0.05)(D)
        return {
            "x": _w((lambda *s: torch.randn(*s, dtype=torch.bfloat16) * 0.05)(B, S, D)),
            "Lambda_re": _w(Lambda_re),
            "Lambda_im": _w(Lambda_im),
            "B_re": _w(B_re),
            "B_im": _w(B_im),
            "C_re": _w(C_re),
            "C_im": _w(C_im),
            "log_dt": _w(log_dt),
            "Dskip": _w(Dskip),
        }
    _A_in = _A_setup()

    def _B_setup():
        B, S, D, F = 1, 2048, 1024, 3584
        return {
            "x": _w((lambda *s: torch.randn(*s, dtype=torch.bfloat16) * 0.05)(B, S, D)),
            "w1": _w((lambda *s: torch.randn(*s, dtype=torch.bfloat16) * 0.8)(D, F) * (2.0 / D) ** 0.5),
            "w3": _w((lambda *s: torch.randn(*s, dtype=torch.bfloat16) * 0.8)(D, F) * (2.0 / D) ** 0.5),
            "w2": _w((lambda *s: torch.randn(*s, dtype=torch.bfloat16) * 0.8)(F, D) * (2.0 / F) ** 0.5),
        }
    _B_in = _B_setup()

    return {
        "A_x": _A_in["x"],
        "A_Lambda_re": _A_in["Lambda_re"],
        "A_Lambda_im": _A_in["Lambda_im"],
        "A_B_re": _A_in["B_re"],
        "A_B_im": _A_in["B_im"],
        "A_C_re": _A_in["C_re"],
        "A_C_im": _A_in["C_im"],
        "A_log_dt": _A_in["log_dt"],
        "A_Dskip": _A_in["Dskip"],
        "B_x": _B_in["x"],
        "B_w1": _B_in["w1"],
        "B_w3": _B_in["w3"],
        "B_w2": _B_in["w2"],
        "gate_w": _w((lambda *s: torch.randn(*s, dtype=torch.bfloat16) * 0.05)(1024) * 16.0),
    }

def reference(A_x, A_Lambda_re, A_Lambda_im, A_B_re, A_B_im, A_C_re, A_C_im, A_log_dt, A_Dskip, B_x, B_w1, B_w3, B_w2, gate_w):
    def _A_ref(x, Lambda_re, Lambda_im, B_re, B_im, C_re, C_im, log_dt, Dskip):
        Bsz, L, H = x.shape
        Lam = Lambda_re + 1j * Lambda_im
        Bmat = B_re + 1j * B_im
        Cmat = C_re + 1j * C_im
        dt = torch.exp(log_dt)
        Lam_bar = torch.exp(dt * Lam)
        B_bar = ((Lam_bar - 1.0) / Lam)[:, None] * Bmat
        Bu = torch.einsum("bld,pd->blp", x + 0j, B_bar)
        h = torch.zeros_like(Bu[:, 0, :])
        hs = []
        for t in range(L):
            h = Lam_bar[None, :] * h + Bu[:, t, :]
            hs.append(h)
        hseq = torch.stack(hs, 1)
        y = 2.0 * torch.einsum("blp,dp->bld", hseq, Cmat).real + x * Dskip[None, None, :]
        return y

    def _B_ref(x, w1, w3, w2):
        gate = torch.einsum("bsd,df->bsf", x, w1)
        up = torch.einsum("bsd,df->bsf", x, w3)
        h = _silu(gate) * up
        out = torch.einsum("bsf,fd->bsd", h, w2)
        return out

    x = A_x
    g = _sigmoid((x * gate_w).sum(-1, keepdims=True))
    a = _A_ref(x, A_Lambda_re, A_Lambda_im, A_B_re, A_B_im, A_C_re, A_C_im, A_log_dt, A_Dskip)
    b = _B_ref(x, B_w1, B_w3, B_w2)
    return g * a + (1 - g) * b
```

```cpp
#include <hip/hip_runtime.h>
#include <hip/hip_bf16.h>
#define VARIANT 1
typedef __hip_bfloat16 bf16;
__device__ inline float b2f(bf16 v){ return __bfloat162float(v); }
__device__ inline float rb(float v){ return __bfloat162float(__float2bfloat16(v)); }
constexpr int S=2048, D=1024, P=1024, F=3584;

__global__ void k_gate(const bf16* x, const bf16* gw, float* g){
  int t = blockIdx.x; float s=0;
  for(int d=threadIdx.x; d<D; d+=64){ float p=b2f(x[t*D+d])*b2f(gw[d]);
    p=rb(p);
    s+=p; }
  for(int o=32;o>=1;o>>=1) s+=__shfl_xor(s,o);
  s=rb(s);
  float gg=1.f/(1.f+expf(-s));
  gg=rb(gg);
  if(threadIdx.x==0) g[t]=gg;
}
__global__ void k_bu(const bf16* x, const bf16* Bre, const bf16* Bim, float* bur, float* bui){
  int p = blockIdx.x*blockDim.x+threadIdx.x; int t=blockIdx.y;
  float sr=0, si=0;
  for(int d=0; d<D; ++d){ float xv=b2f(x[t*D+d]); sr+=xv*b2f(Bre[p*D+d]); si+=xv*b2f(Bim[p*D+d]); }
  bur[t*P+p]=sr; bui[t*P+p]=si;
}
__global__ void k_scan(const bf16* Lre, const bf16* Lim, const bf16* ldt, float* bur, float* bui){
  int p = blockIdx.x*blockDim.x+threadIdx.x;
  float lr=b2f(Lre[p]), li=b2f(Lim[p]); float dt=expf(b2f(ldt[p]));
  dt=rb(dt);
  float er=expf(dt*lr); float br=er*cosf(dt*li), bi=er*sinf(dt*li);
  float nr=br-1.f, ni=bi; float den=lr*lr+li*li;
  float cr=(nr*lr+ni*li)/den, ci=(ni*lr-nr*li)/den;
  float hr=0, hi=0;
  for(int t=0;t<S;++t){ float ar=bur[t*P+p], ai=bui[t*P+p];
    float ur=cr*ar-ci*ai, ui=cr*ai+ci*ar;
    float nhr=br*hr-bi*hi+ur, nhi=br*hi+bi*hr+ui; hr=nhr; hi=nhi; bur[t*P+p]=hr; bui[t*P+p]=hi; }
}
__global__ void k_gu(const bf16* x, const bf16* w1, const bf16* w3, float* h){
  int f = blockIdx.x*blockDim.x+threadIdx.x; int t=blockIdx.y;
  float a=0,b=0;
  for(int d=0; d<D; ++d){ float xv=b2f(x[t*D+d]); a+=xv*b2f(w1[d*F+f]); b+=xv*b2f(w3[d*F+f]); }
  a=rb(a); b=rb(b); float s=rb(a/(1.f+expf(-a))); h[t*F+f]=rb(s*b);
}
__global__ void k_out(const bf16* x, const float* hr, const float* hi, const bf16* Cre, const bf16* Cim, const bf16* Dk, const float* h, const bf16* w2, const float* g, float* out){
  int d = blockIdx.x*blockDim.x+threadIdx.x; int t=blockIdx.y;
  float y=0; for(int p=0;p<P;++p){ y+=hr[t*P+p]*b2f(Cre[d*P+p])-hi[t*P+p]*b2f(Cim[d*P+p]); }
  float sk=b2f(x[t*D+d])*b2f(Dk[d]);
  sk=rb(sk);
  float a=2.f*y+sk;
  float b=0; for(int f=0;f<F;++f) b+=h[t*F+f]*b2f(w2[f*D+d]);
  float gg=g[t];
  b=rb(b); float og=rb(1.f-gg); out[t*D+d]=gg*a+rb(og*b);
}
extern "C" void kernel_launch(void* const* d_in, const int* in_sizes, int n_in, void* d_out, int out_size, void* d_ws, size_t ws_size, hipStream_t stream){
  const bf16* x=(const bf16*)d_in[0]; const bf16* Lre=(const bf16*)d_in[1]; const bf16* Lim=(const bf16*)d_in[2];
  const bf16* Bre=(const bf16*)d_in[3]; const bf16* Bim=(const bf16*)d_in[4]; const bf16* Cre=(const bf16*)d_in[5]; const bf16* Cim=(const bf16*)d_in[6];
  const bf16* ldt=(const bf16*)d_in[7]; const bf16* Dk=(const bf16*)d_in[8];
  const bf16* w1=(const bf16*)d_in[10]; const bf16* w3=(const bf16*)d_in[11]; const bf16* w2=(const bf16*)d_in[12]; const bf16* gw=(const bf16*)d_in[13];
  float* ws=(float*)d_ws; float* g=ws; float* bur=ws+4096; float* bui=bur+S*P; float* h=bui+S*P;
  k_gate<<<S,64,0,stream>>>(x,gw,g);
  k_bu<<<dim3(P/256,S),256,0,stream>>>(x,Bre,Bim,bur,bui);
  k_scan<<<P/64,64,0,stream>>>(Lre,Lim,ldt,bur,bui);
  k_gu<<<dim3(F/256,S),256,0,stream>>>(x,w1,w3,h);
  k_out<<<dim3(D/256,S),256,0,stream>>>(x,bur,bui,Cre,Cim,Dk,h,w2,g,(float*)d_out);
}
```

```cpp
#include <hip/hip_runtime.h>
#include <hip/hip_bf16.h>
#include <stdint.h>

#define LAS __attribute__((address_space(3)))
#define GAS __attribute__((address_space(1)))
typedef unsigned short u16;
typedef unsigned long long u64;
typedef short bf16x8 __attribute__((ext_vector_type(8)));
typedef short s16x4 __attribute__((ext_vector_type(4)));
typedef float f32x4 __attribute__((ext_vector_type(4)));
typedef float f32x2 __attribute__((ext_vector_type(2)));
typedef unsigned u32x4 __attribute__((ext_vector_type(4)));
typedef unsigned u32x2 __attribute__((ext_vector_type(2)));

namespace {
constexpr int S_ = 2048, D_ = 1024, P_ = 1024, F_ = 3584;
constexpr int KS2 = 88;
constexpr size_t WS_GRAN = 0;
constexpr size_t WS_FLAG = 131072;
constexpr size_t WS_ZERO_BYTES = 131072 + 16384;
constexpr size_t WS_G = 262144;
constexpr size_t WS_CH = 262144 + 8192;
constexpr size_t WS_A2 = 1u << 20;
constexpr size_t WS_XB = 32u << 20;
constexpr unsigned TAG = 0x5A5A0001u;
constexpr unsigned SENT = 0xFFFFFFFFu;

__device__ __forceinline__ float bf2f(u16 v) { return __uint_as_float(((unsigned)v) << 16); }
__device__ __forceinline__ unsigned pk_bf16(float lo, float hi) {
    unsigned r; asm("v_cvt_pk_bf16_f32 %0, %1, %2" : "=v"(r) : "v"(lo), "v"(hi)); return r; }
__device__ __forceinline__ f32x2 cmul(f32x2 a, f32x2 b) { return (f32x2){a.x * b.x - a.y * b.y, a.x * b.y + a.y * b.x}; }
__device__ __forceinline__ f32x2 cfma(f32x2 a, f32x2 b, f32x2 c) { return (f32x2){a.x * b.x - a.y * b.y + c.x, a.x * b.y + a.y * b.x + c.y}; }

#define STAMP(i)
#define STAMP_DECL
#define STAMP_OUT(kern)
#define WAIT_V(n) asm volatile("s_waitcnt vmcnt(" #n ")" ::: "memory")
#define WAIT_L(n) asm volatile("s_waitcnt lgkmcnt(" #n ")" ::: "memory")
#define BAR __builtin_amdgcn_s_barrier()
#define SCHED __builtin_amdgcn_sched_barrier(0)
#define DSR128(dst, addr, off) asm volatile("ds_read_b128 %0, %1 offset:%2" : "=v"(dst) : "v"(addr), "i"(off))
#define DSRTR(dst, addr, off) asm volatile("ds_read_b64_tr_b16 %0, %1 offset:%2" : "=v"(dst) : "v"(addr), "i"(off))
#define CAT(lo, hi) __builtin_shufflevector(lo, hi, 0, 1, 2, 3, 4, 5, 6, 7)
#define MKRS(p, bytes) __builtin_amdgcn_make_buffer_rsrc((void*)(p), 0, (int)(bytes), 0x00020000)
#define BLDS(rs, dst, voff, soff) __builtin_amdgcn_raw_ptr_buffer_load_lds((rs), (LAS void*)(dst), 16, (int)(voff), (int)(soff), 0, 0)

__global__ __launch_bounds__(512) void k0_prep(const u16* __restrict__ x, const u16* __restrict__ gw, const u16* __restrict__ lre,
                                               const u16* __restrict__ lim, const u16* __restrict__ ldt, unsigned char* __restrict__ ws) {
    const int tid = threadIdx.x, wid = tid >> 6, lane = tid & 63, bid = blockIdx.x;
    const int row = bid * 8 + wid;
    const u32x4* xr = (const u32x4*)(x + (size_t)row * D_);
    const u32x4* gr = (const u32x4*)gw;
    float s = 0.f;
#pragma unroll
    for (int i = 0; i < 2; ++i) {
        const u32x4 a = xr[lane + 64 * i], b = gr[lane + 64 * i];
#pragma unroll
        for (int j = 0; j < 4; ++j) {
            s += __uint_as_float(a[j] << 16) * __uint_as_float(b[j] << 16);
            s += __uint_as_float(a[j] & 0xffff0000u) * __uint_as_float(b[j] & 0xffff0000u);
        }
    }
#pragma unroll
    for (int o = 32; o >= 1; o >>= 1) s += __shfl_xor(s, o);
    if (lane == 0) ((float*)(ws + WS_G))[row] = 1.f / (1.f + __expf(-s));
    if (tid < 36) ((u32x4*)(ws + (size_t)bid * 576))[tid] = (u32x4){0u, 0u, 0u, 0u};
    if (bid < 2) {
        const int p = bid * 512 + tid;
        const float lr = bf2f(lre[p]), li = bf2f(lim[p]), dt = expf(bf2f(ldt[p]));
        const float er = expf(dt * lr); float sn, cs; sincosf(dt * li, &sn, &cs);
        const float br = er * cs, bi = er * sn;
        const float nr = br - 1.f, ni = bi, den = lr * lr + li * li;
        ((f32x4*)(ws + WS_CH))[p] = (f32x4){br, bi, (nr * lr + ni * li) / den, (ni * lr - nr * li) / den};
    }
}

constexpr int K1_A = 0, K1_W1 = 32768, K1_W3 = 32768 + 14336 + 128, K1_BS = K1_W3 + 14336, K1_BUF = K1_BS + 8192;
constexpr int K1_X0 = 2 * K1_BUF;
constexpr int K1_G = K1_X0, K1_CH = K1_X0 + 1024, K1_AGG = K1_X0 + 2048, K1_XC = K1_X0 + 3072, K1_LDS = K1_X0 + 3328;
constexpr int RS = 368;

__device__ __forceinline__ size_t a2_addr(int R, int cf) {
    const int rt = R >> 7, rr = R & 127, s = cf >> 3, c = cf & 7;
    return ((size_t)(rt * KS2 + s) * 128 + rr) * 128 + ((c ^ ((rr >> 1) & 7)) << 4);
}

__global__ __launch_bounds__(512, 2) void k1_gemm(const u16* __restrict__ x, const u16* __restrict__ w1, const u16* __restrict__ w3,
                                                  const u16* __restrict__ Bre, const u16* __restrict__ Bim, unsigned char* __restrict__ ws) {
    extern __shared__ __attribute__((aligned(16))) unsigned char smem[];
    LAS unsigned char* lds = (LAS unsigned char*)smem;
    const int tid = threadIdx.x, wid = __builtin_amdgcn_readfirstlane(tid >> 6), lane = tid & 63;
    const int wm = wid & 3, wn = wid >> 2, fr = lane & 15, fq = lane >> 4;
    const int bid = blockIdx.x, xcd = bid & 7, rr_ = bid >> 3, cg = xcd * 4 + (rr_ & 3), pm = rr_ >> 2;

    STAMP_DECL; STAMP(0);
    const auto rsX = MKRS(x, S_ * D_ * 2);
    const int sA = pm * 524288 + wid * 65536;
    unsigned voffA[2];
#pragma unroll
    for (int i = 0; i < 2; ++i) voffA[i] = (unsigned)((lane >> 3) * 2048 + (((lane & 7) ^ ((i * 4 + (lane >> 4)) & 7)) << 4));
    unsigned voffF[4]; int ldsF[4];
    const auto rsF0 = MKRS(w1, D_ * F_ * 2);
    const auto rsF1 = MKRS(wid + 8 < 14 ? w1 : w3, D_ * F_ * 2);
    const auto rsF2 = MKRS(w3, D_ * F_ * 2);
#pragma unroll
    for (int jj = 0; jj < 4; ++jj) {
        int j = wid + 8 * jj; if (j >= 28) j = 16 + wid;
        const int mat = j / 14, v = j % 14, idx = 64 * v + lane, k = idx / 14, pc = idx % 14;
        voffF[jj] = (unsigned)(k * 7168 + ((pc ^ ((k >> 3) & 1)) << 4));
        ldsF[jj] = (mat ? K1_W3 : K1_W1) + v * 1024;
    }
    const unsigned voffS = (unsigned)((8 * (wid & 1) + (lane >> 3)) * 2048 + (((lane & 7) ^ ((4 * (wid & 1) + (lane >> 4)) & 7)) << 4));
    const auto rsS = MKRS(((wid >> 1) & 1) ? Bim : Bre, P_ * D_ * 2);
    const int sS = (cg * 32 + 16 * (wid >> 2)) * 2048;

#define STAGE_A(par, t) do { _Pragma("unroll") for (int _i = 0; _i < 4; ++_i) \
        BLDS(rsX, lds + (par) * K1_BUF + K1_A + (wid * 4 + _i) * 1024, voffA[_i & 1], sA + (t) * 128 + _i * 16384); } while (0)
#define STAGE_B(par, t) do { const int _sf = cg * 224 + (t) * (64 * 7168); \
        BLDS(rsF0, lds + (par) * K1_BUF + ldsF[0], voffF[0], _sf); \
        BLDS(rsF1, lds + (par) * K1_BUF + ldsF[1], voffF[1], _sf); \
        BLDS(rsF2, lds + (par) * K1_BUF + ldsF[2], voffF[2], _sf); \
        if (wn == 0) BLDS(rsF2, lds + (par) * K1_BUF + ldsF[3], voffF[3], _sf); \
        BLDS(rsS, lds + (par) * K1_BUF + K1_BS + wid * 1024, voffS, sS + (t) * 128); } while (0)

    const unsigned lb = (unsigned)(size_t)lds;
    const int sw = fq ^ (fr >> 1);
    const int tq = (lane & 15) >> 2, tp = lane & 3;
    const int tbase = K1_W1 + (8 * fq + tq) * 224 + (tp & 1) * (K1_W3 - K1_W1) + 8 * (tp >> 1);
    unsigned aB[2][2], sB[2][2], tB[2][2];
#pragma unroll
    for (int p = 0; p < 2; ++p) {
        aB[p][0] = lb + p * K1_BUF + K1_A + (wm * 64 + fr) * 128 + (sw << 4); aB[p][1] = lb + p * K1_BUF + K1_A + (wm * 64 + fr) * 128 + ((sw ^ 4) << 4);
        sB[p][0] = lb + p * K1_BUF + K1_BS + fr * 128 + (sw << 4); sB[p][1] = lb + p * K1_BUF + K1_BS + fr * 128 + ((sw ^ 4) << 4);
        tB[p][0] = lb + p * K1_BUF + tbase + ((fq & 1) << 4); tB[p][1] = lb + p * K1_BUF + tbase + (((fq & 1) ^ 1) << 4);
    }

    f32x4 acc[4][9];
#pragma unroll
    for (int m = 0; m < 4; ++m)
#pragma unroll
        for (int n = 0; n < 9; ++n) acc[m][n] = (f32x4){0.f, 0.f, 0.f, 0.f};
    bf16x8 Af[4][2], Bs[3][2]; s16x4 Bl[3][2], Bh[3][2];

#define LDA(par) do { _Pragma("unroll") for (int _m = 0; _m < 4; ++_m) { \
        DSR128(Af[_m][0], aB[par][0], _m * 2048); DSR128(Af[_m][1], aB[par][1], _m * 2048); } } while (0)
#define LDF(par, slot, i) do { \
        DSRTR(Bl[slot][0], tB[par][(i) & 1], ((i) >> 1) * 32); DSRTR(Bh[slot][0], tB[par][(i) & 1], 4 * 224 + ((i) >> 1) * 32); \
        DSRTR(Bl[slot][1], tB[par][(i) & 1], 32 * 224 + ((i) >> 1) * 32); DSRTR(Bh[slot][1], tB[par][(i) & 1], 36 * 224 + ((i) >> 1) * 32); } while (0)
#define LDS_(par, slot, tau) do { DSR128(Bs[slot][0], sB[par][0], (tau) * 2048); DSR128(Bs[slot][1], sB[par][1], (tau) * 2048); } while (0)
#define WA asm volatile("" : "+v"(Af[0][0]), "+v"(Af[0][1]), "+v"(Af[1][0]), "+v"(Af[1][1]), "+v"(Af[2][0]), "+v"(Af[2][1]), "+v"(Af[3][0]), "+v"(Af[3][1]))
#define WF(s) asm volatile("" : "+v"(Bl[s][0]), "+v"(Bh[s][0]), "+v"(Bl[s][1]), "+v"(Bh[s][1]))
#define WS_(s) asm volatile("" : "+v"(Bs[s][0]), "+v"(Bs[s][1]))
#define MMA3(n0, NS) do { __builtin_amdgcn_s_setprio(1); \
        _Pragma("unroll") for (int _k = 0; _k < 2; ++_k) _Pragma("unroll") for (int _m = 0; _m < 4; ++_m) _Pragma("unroll") for (int _n = 0; _n < 3; ++_n) { \
            if ((n0) + _n >= (NS)) acc[_m][(n0) + _n] = __builtin_amdgcn_mfma_f32_16x16x32_bf16(Af[_m][_k], Bs[_n][_k], acc[_m][(n0) + _n], 0, 0, 0); \
            else acc[_m][(n0) + _n] = __builtin_amdgcn_mfma_f32_16x16x32_bf16(CAT(Bl[_n][_k], Bh[_n][_k]), Af[_m][_k], acc[_m][(n0) + _n], 0, 0, 0); } \
        __builtin_amdgcn_s_setprio(0); } while (0)

    { const auto rsW = MKRS(ws, 1u << 20);
      if (wid == 0) BLDS(rsW, lds + K1_G, lane * 16, WS_G + pm * 1024);
      if (wid == 1) BLDS(rsW, lds + K1_CH, (lane & 31) * 16, WS_CH + cg * 512); }
    { u32x4* xb = (u32x4*)(ws + WS_XB + (size_t)bid * 32768) + tid;
#pragma unroll
      for (int i = 0; i < 4; ++i) xb[i * 512] = (u32x4){SENT, SENT, SENT, SENT}; }
    STAGE_A(0, 0); STAGE_B(0, 0); STAGE_A(1, 1);
    WAIT_V(4); BAR;
    STAMP(1);
    if (wn == 0) {
#define STEP0(par, t) do { \
        LDA(par); LDF(par, 0, 0); LDF(par, 1, 1); LDF(par, 2, 2); \
        BAR; WAIT_L(0); WA; WF(0); WF(1); WF(2); SCHED; MMA3(0, 7); BAR; SCHED; \
        LDF(par, 0, 3); LDF(par, 1, 4); LDF(par, 2, 5); if ((t) + 1 < 16) STAGE_B((par) ^ 1, (t) + 1); \
        BAR; WAIT_L(0); WF(0); WF(1); WF(2); SCHED; MMA3(3, 7); BAR; SCHED; \
        LDF(par, 0, 6); LDS_(par, 1, 0); LDS_(par, 2, 1); if ((t) + 2 < 16) STAGE_A(par, (t) + 2); \
        BAR; WAIT_L(0); WF(0); WS_(1); WS_(2); SCHED; MMA3(6, 7); if ((t) + 2 < 16) WAIT_V(4); else WAIT_V(0); BAR; SCHED; } while (0)
        for (int t = 0; t < 16; t += 2) { STEP0(0, t); STEP0(1, t + 1); }
    } else {
        BAR;
#define STEP1(par, t, LASTBAR) do { \
        LDA(par); LDF(par, 0, 7); LDF(par, 1, 8); LDF(par, 2, 9); if ((t) + 1 < 16) STAGE_B((par) ^ 1, (t) + 1); \
        BAR; WAIT_L(0); WA; WF(0); WF(1); WF(2); SCHED; MMA3(0, 7); BAR; SCHED; \
        LDF(par, 0, 10); LDF(par, 1, 11); LDF(par, 2, 12); if ((t) + 2 < 16) STAGE_A(par, (t) + 2); \
        BAR; WAIT_L(0); WF(0); WF(1); WF(2); SCHED; MMA3(3, 7); BAR; SCHED; \
        LDF(par, 0, 13); LDS_(par, 1, 2); LDS_(par, 2, 3); if ((t) + 2 < 16) WAIT_V(4); else WAIT_V(0); \
        BAR; WAIT_L(0); WF(0); WS_(1); WS_(2); SCHED; MMA3(6, 7); if (LASTBAR) BAR; SCHED; } while (0)
        for (int t = 0; t < 16; t += 2) { STEP1(0, t, true); STEP1(1, t + 1, t + 1 < 15); }
    }

    STAMP(2);
    {
    int tz = threadIdx.x; asm volatile("" : "+v"(tz));
    const int tid = tz, lane = tz & 63, fr = lane & 15, fq = lane >> 4;
    const LAS float* sg = (const LAS float*)(lds + K1_G);
    unsigned char* A2 = ws + WS_A2;
#define FFN_EPI(n, i) do { _Pragma("unroll") for (int _m = 0; _m < 4; ++_m) { \
        const f32x4 _v = acc[_m][n]; \
        const auto _s0 = __builtin_amdgcn_permlane16_swap(__float_as_uint(_v[0]), __float_as_uint(_v[2]), false, false); \
        const auto _s1 = __builtin_amdgcn_permlane16_swap(__float_as_uint(_v[1]), __float_as_uint(_v[3]), false, false); \
        const f32x2 _g = (f32x2){__uint_as_float(_s0[0]), __uint_as_float(_s1[0])}, _u = (f32x2){__uint_as_float(_s0[1]), __uint_as_float(_s1[1])}; \
        const f32x2 _t = _g * (-1.44269504089f); \
        const f32x2 _d = (f32x2){__builtin_amdgcn_exp2f(_t.x), __builtin_amdgcn_exp2f(_t.y)} + 1.0f; \
        const f32x2 _r = (f32x2){__builtin_amdgcn_rcpf(_d.x), __builtin_amdgcn_rcpf(_d.y)}; \
        const f32x2 _h = (_g * _u) * (_r * omg[_m]); \
        *(LAS unsigned*)(lds + (wm * 64 + _m * 16 + fr) * RS + (i) * 16 + fq * 4) = pk_bf16(_h.x, _h.y); } } while (0)

    const LAS f32x4* sch = (const LAS f32x4*)(lds + K1_CH);
    LAS f32x2* sagg = (LAS f32x2*)(lds + K1_AGG);
    f32x2 lam, l16, l64, lq, cf, ex[4], T[4];
    {
        const f32x4 c = sch[wn * 16 + fr];
        lam = (f32x2){c[0], c[1]}; cf = (f32x2){c[2], c[3]};
        const f32x2 l2 = cmul(lam, lam), l4 = cmul(l2, l2), l8 = cmul(l4, l4); l16 = cmul(l8, l8);
        const f32x2 l32 = cmul(l16, l16); l64 = cmul(l32, l32);
        const f32x2 l12 = cmul(l4, l8);
        lq = fq == 0 ? (f32x2){1.f, 0.f} : (fq == 1 ? l4 : (fq == 2 ? l8 : l12));
        f32x2 W = (f32x2){0.f, 0.f};
#pragma unroll
        for (int m = 0; m < 4; ++m) {
            f32x2 a = (f32x2){0.f, 0.f};
#pragma unroll
            for (int j = 0; j < 4; ++j) a = cfma(lam, a, cmul(cf, (f32x2){acc[m][7][j], acc[m][8][j]}));
            f32x2 x0, x1, x2, x3;
            { const auto p = __builtin_amdgcn_permlane16_swap(__float_as_uint(a.x), __float_as_uint(a.x), false, false);
              const auto q = __builtin_amdgcn_permlane32_swap(p[0], p[0], false, false), r = __builtin_amdgcn_permlane32_swap(p[1], p[1], false, false);
              x0.x = __uint_as_float(q[0]); x2.x = __uint_as_float(q[1]); x1.x = __uint_as_float(r[0]); x3.x = __uint_as_float(r[1]); }
            { const auto p = __builtin_amdgcn_permlane16_swap(__float_as_uint(a.y), __float_as_uint(a.y), false, false);
              const auto q = __builtin_amdgcn_permlane32_swap(p[0], p[0], false, false), r = __builtin_amdgcn_permlane32_swap(p[1], p[1], false, false);
              x0.y = __uint_as_float(q[0]); x2.y = __uint_as_float(q[1]); x1.y = __uint_as_float(r[0]); x3.y = __uint_as_float(r[1]); }
            const f32x2 e2 = cfma(l4, x0, x1), e3 = cfma(l4, e2, x2);
            T[m] = cfma(l4, e3, x3);
            ex[m] = fq == 0 ? (f32x2){0.f, 0.f} : (fq == 1 ? x0 : (fq == 2 ? e2 : e3));
            W = cfma(l16, W, T[m]);
        }
        if (fq == 0) sagg[wm * 32 + wn * 16 + fr] = W;
    }
    STAMP(3); WAIT_L(0); BAR;
    STAMP(4);
    GAS u64* gran = (GAS u64*)(ws + WS_GRAN) + (size_t)cg * 8 * 64;
    f32x2 l256 = (f32x2){0.f, 0.f};
    if (wid == 0) {
        const int ch = lane & 31, part = lane >> 5;
        const f32x4 c = sch[ch];
        f32x2 p = (f32x2){c[0], c[1]};
#pragma unroll
        for (int i = 0; i < 6; ++i) p = cmul(p, p);
        f32x2 a = sagg[ch]; a = cfma(p, a, sagg[32 + ch]); a = cfma(p, a, sagg[64 + ch]); a = cfma(p, a, sagg[96 + ch]);
        l256 = cmul(p, p); l256 = cmul(l256, l256);
        if (pm < 7) __hip_atomic_store(gran + pm * 64 + ch * 2 + part, ((u64)TAG << 32) | __float_as_uint(part ? a.y : a.x), __ATOMIC_RELAXED, __HIP_MEMORY_SCOPE_AGENT);
    }
    {
        float omg[4];
#pragma unroll
        for (int m = 0; m < 4; ++m) omg[m] = 1.f - sg[wm * 64 + m * 16 + fr];
#pragma unroll
        for (int n = 0; n < 7; ++n) FFN_EPI(n, wn * 7 + n);
    }
    if (wid == 0) {
        const int ch = lane & 31, part = lane >> 5;
        f32x2 X = (f32x2){0.f, 0.f};
        if (pm > 0) {
            u64 v[7];
            for (unsigned spins = 0;; ++spins) {
                bool ok = true;
#pragma unroll
                for (int j = 0; j < 7; ++j) { const int jj = j < pm ? j : pm - 1;
                    v[j] = __hip_atomic_load(gran + jj * 64 + ch * 2 + part, __ATOMIC_RELAXED, __HIP_MEMORY_SCOPE_AGENT); ok &= (unsigned)(v[j] >> 32) == TAG; }
                if (__all(ok) || spins > (1u << 20)) break;
                __builtin_amdgcn_s_sleep(1);
            }
#pragma unroll
            for (int j = 0; j < 7; ++j) { const float mine = __uint_as_float((unsigned)v[j]), other = __shfl_xor(mine, 32);
                const f32x2 aj = part ? (f32x2){other, mine} : (f32x2){mine, other};
                if (j < pm) X = cfma(l256, X, aj); }
        }
        if (lane < 32) ((LAS f32x2*)(lds + K1_XC))[ch] = X;
    }
    STAMP(5); WAIT_L(0); BAR;
    STAMP(6);
    {
        f32x2 c = ((const LAS f32x2*)(lds + K1_XC))[wn * 16 + fr];
        for (int w = 0; w < wm; ++w) c = cfma(l64, c, sagg[w * 32 + wn * 16 + fr]);
#pragma unroll
        for (int m = 0; m < 4; ++m) {
            f32x2 h = cfma(lq, c, ex[m]);
            const f32x4 g4 = *(const LAS f32x4*)(sg + wm * 64 + m * 16 + fq * 4);
#pragma unroll
            for (int j = 0; j < 4; ++j) {
                h = cfma(lam, h, cmul(cf, (f32x2){acc[m][7][j], acc[m][8][j]}));
                const float s2 = 2.f * g4[j];
                const unsigned pk = pk_bf16(s2 * h.x, -s2 * h.y);
                LAS unsigned char* dst = lds + (wm * 64 + m * 16 + fq * 4 + j) * RS + 224 + (wn * 16 + fr) * 2;
                *(LAS u16*)dst = (u16)(pk & 0xffffu);
                *(LAS u16*)(dst + 64) = (u16)(pk >> 16);
            }
            c = cfma(l16, c, T[m]);
        }
    }
#pragma unroll
    for (int it = 0; it < 7; ++it) { const int idx = it * 512 + tid, row = idx / 14, j = idx % 14;
        const u32x4 v = *(const LAS u32x4*)(lds + row * RS + j * 16);
        *(u32x4*)(A2 + a2_addr(pm * 256 + row, cg * 14 + j)) = v; }
    STAMP(7); WAIT_L(0); BAR;
    STAMP(8);
#pragma unroll
    for (int it = 0; it < 4; ++it) { const int idx = it * 512 + tid, row = idx >> 3, j = idx & 7;
        const u32x4 v = *(const LAS u32x4*)(lds + row * RS + 224 + j * 16);
        *(u32x4*)(A2 + a2_addr(pm * 256 + row, 448 + (j >> 2) * 128 + cg * 4 + (j & 3))) = v; }
    STAMP(9);
    STAMP(10); STAMP_OUT(0);
    }
}

constexpr int K2_ST = 32768, K2_NST = 5, K2_LDS = K2_NST * K2_ST;

__global__ __launch_bounds__(512, 2) void k2_gemm(const u16* __restrict__ x, const u16* __restrict__ w2, const u16* __restrict__ Cre,
                                                  const u16* __restrict__ Cim, const u16* __restrict__ dsk, unsigned char* __restrict__ ws,
                                                  float* __restrict__ out) {
    extern __shared__ __attribute__((aligned(16))) unsigned char smem[];
    LAS unsigned char* lds = (LAS unsigned char*)smem;
    const int tid = threadIdx.x, wid = __builtin_amdgcn_readfirstlane(tid >> 6), lane = tid & 63;
    const int grp = wid >> 2, wm = (wid >> 1) & 1, wn = wid & 1, fr = lane & 15, fq = lane >> 4;
    const int bid = blockIdx.x, xcd = bid & 7, r_ = bid >> 3, kh = xcd >> 2, sub = xcd & 3;
    const int rt = (sub >> 1) * 8 + (r_ >> 2), ct = (sub & 1) * 4 + (r_ & 3);
    const int s0 = kh * 44;
    constexpr int NS = 44;

    STAMP_DECL; STAMP(0);
    const int w4 = wid & 3;
    const auto rsA = MKRS(ws + WS_A2, S_ * 5632 * 2);
    const int sA = (rt * KS2 + s0) * 16384 + w4 * 4096;
    const unsigned voffL = lane * 16;
    unsigned voffW[2], voffC[2];
#pragma unroll
    for (int i = 0; i < 2; ++i) {
        voffW[i] = (unsigned)((lane >> 4) * 2048 + ((((lane & 15) >> 1) ^ ((lane >> 4) | (i << 2))) << 5) + ((lane & 1) << 4));
        voffC[i] = (unsigned)((lane >> 3) * 2048 + (((lane & 7) ^ ((i * 4 + (lane >> 4)) & 7)) << 4)); }
    const auto rsW = MKRS(w2, F_ * D_ * 2);
    const int sW = w4 * 16 * 2048 + ct * 256 + s0 * (64 * 2048);
    const auto rsCr = MKRS(Cre, D_ * P_ * 2); const auto rsCi = MKRS(Cim, D_ * P_ * 2);
    const int sC = (ct * 128 + w4 * 32) * 2048;
#define BLDS2 BLDS
#define STAGE2(sl, so) do { LAS unsigned char* _d = lds + (so) + w4 * 4096; \
        _Pragma("unroll") for (int _i = 0; _i < 4; ++_i) BLDS2(rsA, _d + _i * 1024, voffL, sA + (sl) * 16384 + _i * 1024); \
        if (s0 + (sl) < 56) { _Pragma("unroll") for (int _i = 0; _i < 4; ++_i) BLDS2(rsW, _d + 16384 + _i * 1024, voffW[_i >> 1], sW + (sl) * (64 * 2048) + _i * 8192); } \
        else { const auto _r = (sl) < 28 ? rsCr : rsCi; \
               _Pragma("unroll") for (int _i = 0; _i < 4; ++_i) BLDS2(_r, _d + 16384 + _i * 1024, voffC[_i & 1], sC + (((sl) - 12) & 15) * 128 + _i * 16384); } } while (0)

    const unsigned lb = (unsigned)(size_t)lds;
    const int sw = (fq ^ (fr >> 1)) << 4;
    const unsigned aoff0 = lb + (wm * 64 + fr) * 128 + sw, aoff1 = lb + (wm * 64 + fr) * 128 + (sw ^ 64);
    const unsigned coff0 = lb + 16384 + (wn * 64 + fr) * 128 + sw, coff1 = lb + 16384 + (wn * 64 + fr) * 128 + (sw ^ 64);
    const int tq = (lane & 15) >> 2, tp = lane & 3, fl = tq | ((fq & 1) << 2);
    unsigned toff[4];
#pragma unroll
    for (int n = 0; n < 4; ++n) toff[n] = lb + 16384 + (8 * fq + tq) * 256 + (((wn * 4 + n) ^ fl) << 5) + 8 * tp;

    f32x4 acc[4][4];
#pragma unroll
    for (int m = 0; m < 4; ++m)
#pragma unroll
        for (int n = 0; n < 4; ++n) acc[m][n] = (f32x4){0.f, 0.f, 0.f, 0.f};
    bf16x8 Af[4][2], Bf[4][2]; s16x4 Bl[4][2], Bh[4][2];

    if (grp) { STAGE2(0, 0); STAGE2(2, 2 * K2_ST); WAIT_V(8); } else STAGE2(1, K2_ST);
    BAR;
    if (grp) BAR;
    STAMP(1);
    int sl = grp, rdo = grp * K2_ST, sto = (grp + 3) * K2_ST;
#define K2_ADV do { sl += 2; rdo += 2 * K2_ST; if (rdo >= K2_LDS) rdo -= K2_LDS; sto += 2 * K2_ST; if (sto >= K2_LDS) sto -= K2_LDS; } while (0)
#define K2_MMA(BEXPR) do { __builtin_amdgcn_s_setprio(1); \
        _Pragma("unroll") for (int k = 0; k < 2; ++k) _Pragma("unroll") for (int m = 0; m < 4; ++m) _Pragma("unroll") for (int n = 0; n < 4; ++n) \
            acc[m][n] = __builtin_amdgcn_mfma_f32_16x16x32_bf16(BEXPR, Af[m][k], acc[m][n], 0, 0, 0); \
        __builtin_amdgcn_s_setprio(0); } while (0)
#define K2_DMA(TY, i) do { if ((TY) != 2) { LAS unsigned char* _d = lds + sto + w4 * 4096; \
        if ((i) < 4) BLDS2(rsA, _d + (i) * 1024, voffL, sA + (sl + 3) * 16384 + (i) * 1024); \
        else if ((TY) == 0) BLDS2(rsW, _d + 16384 + ((i) - 4) * 1024, voffW[((i) - 4) >> 1], sW + (sl + 3) * (64 * 2048) + ((i) - 4) * 8192); \
        else BLDS2(rsC, _d + 16384 + ((i) - 4) * 1024, voffC[(i) & 1], sC + ((sl + 3 - 12) & 15) * 128 + ((i) - 4) * 16384); } } while (0)
#define K2_STEP(RT, TY) do { \
        const unsigned a0 = aoff0 + rdo, a1 = aoff1 + rdo; const auto rsC = sl + 3 < 28 ? rsCr : rsCi; \
        if ((RT) == 0) { \
            _Pragma("unroll") for (int n = 0; n < 4; ++n) { const unsigned t = toff[n] + rdo; \
                K2_DMA(TY, 2 * n); DSRTR(Bl[n][0], t, 0); DSRTR(Bh[n][0], t, 1024); DSR128(Af[n][0], a0, n * 2048); \
                K2_DMA(TY, 2 * n + 1); DSRTR(Bl[n][1], t, 8192); DSRTR(Bh[n][1], t, 9216); DSR128(Af[n][1], a1, n * 2048); } \
        } else { const unsigned c0 = coff0 + rdo, c1 = coff1 + rdo; \
            _Pragma("unroll") for (int n = 0; n < 4; ++n) { \
                K2_DMA(TY, 2 * n); DSR128(Bf[n][0], c0, n * 2048); DSR128(Af[n][0], a0, n * 2048); \
                K2_DMA(TY, 2 * n + 1); DSR128(Bf[n][1], c1, n * 2048); DSR128(Af[n][1], a1, n * 2048); } } \
        if ((TY) != 2) WAIT_V(8); else WAIT_V(0); \
        BAR; WAIT_L(0); \
        asm volatile("" : "+v"(Af[0][0]), "+v"(Af[0][1]), "+v"(Af[1][0]), "+v"(Af[1][1]), "+v"(Af[2][0]), "+v"(Af[2][1]), "+v"(Af[3][0]), "+v"(Af[3][1])); \
        if ((RT) == 0) { \
            asm volatile("" : "+v"(Bl[0][0]), "+v"(Bh[0][0]), "+v"(Bl[1][0]), "+v"(Bh[1][0]), "+v"(Bl[2][0]), "+v"(Bh[2][0]), "+v"(Bl[3][0]), "+v"(Bh[3][0]), \
                              "+v"(Bl[0][1]), "+v"(Bh[0][1]), "+v"(Bl[1][1]), "+v"(Bh[1][1]), "+v"(Bl[2][1]), "+v"(Bh[2][1]), "+v"(Bl[3][1]), "+v"(Bh[3][1])); \
            SCHED; K2_MMA(CAT(Bl[n][k], Bh[n][k])); \
        } else { \
            asm volatile("" : "+v"(Bf[0][0]), "+v"(Bf[0][1]), "+v"(Bf[1][0]), "+v"(Bf[1][1]), "+v"(Bf[2][0]), "+v"(Bf[2][1]), "+v"(Bf[3][0]), "+v"(Bf[3][1])); \
            SCHED; K2_MMA(Bf[n][k]); } \
        SCHED; K2_ADV; BAR; SCHED; } while (0)
    if (kh == 0) {
        for (; sl < NS - 3;) K2_STEP(0, 0);
        for (; sl < NS;) K2_STEP(0, 2);
    } else {
        for (; sl < 9;) K2_STEP(0, 0);
        for (; sl < 12;) K2_STEP(0, 1);
        for (; sl < NS - 3;) K2_STEP(1, 1);
        for (; sl < NS;) K2_STEP(1, 2);
    }
    STAMP(2);
    if (grp == 0) BAR;
    STAMP(3);
    {
        LAS f32x4* xs = (LAS f32x4*)(lds + ((wid & 3) * 2 + grp) * 8192);
        const LAS f32x4* xr = (const LAS f32x4*)(lds + ((wid & 3) * 2 + (grp ^ 1)) * 8192);
        f32x4 r[2][4];
        if (grp) {
#pragma unroll
            for (int mm = 0; mm < 2; ++mm)
#pragma unroll
                for (int n = 0; n < 4; ++n) { xs[(mm * 4 + n) * 64 + lane] = acc[mm][n]; r[mm][n] = acc[2 + mm][n]; }
        } else {
#pragma unroll
            for (int mm = 0; mm < 2; ++mm)
#pragma unroll
                for (int n = 0; n < 4; ++n) { xs[(mm * 4 + n) * 64 + lane] = acc[2 + mm][n]; r[mm][n] = acc[mm][n]; }
        }
        const int mt = 2 * grp + kh;
        const int row = rt * 128 + wm * 64 + mt * 16 + fr;
        const float g = ((const float*)(ws + WS_G))[row];
        u32x2 xv[4], dv[4];
#pragma unroll
        for (int n = 0; n < 4; ++n) { const int col = ct * 128 + wn * 64 + n * 16 + fq * 4;
            xv[n] = *(const u32x2*)(x + (size_t)row * D_ + col); dv[n] = *(const u32x2*)(dsk + col); }
        WAIT_L(0); BAR;
#pragma unroll
        for (int mm = 0; mm < 2; ++mm)
#pragma unroll
            for (int n = 0; n < 4; ++n) r[mm][n] += xr[(mm * 4 + n) * 64 + lane];
        f32x4 mine[4], give[4];
        if (kh) {
#pragma unroll
            for (int n = 0; n < 4; ++n) { mine[n] = r[1][n]; give[n] = r[0][n]; }
        } else {
#pragma unroll
            for (int n = 0; n < 4; ++n) { mine[n] = r[0][n]; give[n] = r[1][n]; }
        }
        const int pbid = bid ^ 4;
        const auto rs = MKRS(ws + WS_XB, 256 * 32768);
#pragma unroll
        for (int n = 0; n < 4; ++n)
            __builtin_amdgcn_raw_buffer_store_b128(__builtin_bit_cast(u32x4, give[n]), rs, bid * 32768 + wid * 4096 + n * 1024 + lane * 16, 0, 16);
        STAMP(4);
#pragma unroll
        for (int n = 0; n < 4; ++n) {
            mine[n][0] += g * __uint_as_float(xv[n][0] << 16) * __uint_as_float(dv[n][0] << 16);
            mine[n][1] += g * __uint_as_float(xv[n][0] & 0xffff0000u) * __uint_as_float(dv[n][0] & 0xffff0000u);
            mine[n][2] += g * __uint_as_float(xv[n][1] << 16) * __uint_as_float(dv[n][1] << 16);
            mine[n][3] += g * __uint_as_float(xv[n][1] & 0xffff0000u) * __uint_as_float(dv[n][1] & 0xffff0000u); }
        u32x4 pv[4];
        const unsigned char* pp = ws + WS_XB + (size_t)pbid * 32768 + wid * 4096 + lane * 16;
        for (unsigned spins = 0;; ++spins) {
            asm volatile("global_load_dwordx4 %0, %4, off sc1\n\tglobal_load_dwordx4 %1, %4, off offset:1024 sc1\n\t"
                         "global_load_dwordx4 %2, %4, off offset:2048 sc1\n\tglobal_load_dwordx4 %3, %4, off offset:3072 sc1\n\ts_waitcnt vmcnt(0)"
                         : "=&v"(pv[0]), "=&v"(pv[1]), "=&v"(pv[2]), "=&v"(pv[3]) : "v"(pp) : "memory");
            bool ok = true;
#pragma unroll
            for (int n = 0; n < 4; ++n) ok &= (pv[n][0] != SENT) & (pv[n][1] != SENT) & (pv[n][2] != SENT) & (pv[n][3] != SENT);
            if (__all(ok) || spins > (1u << 18)) break;
            __builtin_amdgcn_s_sleep(1);
        }
        STAMP(5);
#pragma unroll
        for (int n = 0; n < 4; ++n)
            *(f32x4*)(out + (size_t)row * D_ + ct * 128 + wn * 64 + n * 16 + fq * 4) = mine[n] + __builtin_bit_cast(f32x4, pv[n]);
        STAMP(6);
        STAMP(7); STAMP_OUT(1);
    }
}
}

extern "C" void kernel_launch(void* const* d_in, const int* in_sizes, int n_in, void* d_out, int out_size, void* d_ws, size_t ws_size,
                              hipStream_t stream) {
    const u16* x = (const u16*)d_in[0]; const u16* lre = (const u16*)d_in[1]; const u16* lim = (const u16*)d_in[2];
    const u16* Bre = (const u16*)d_in[3]; const u16* Bim = (const u16*)d_in[4]; const u16* Cre = (const u16*)d_in[5]; const u16* Cim = (const u16*)d_in[6];
    const u16* ldt = (const u16*)d_in[7]; const u16* dsk = (const u16*)d_in[8];
    const u16* w1 = (const u16*)d_in[10]; const u16* w3 = (const u16*)d_in[11]; const u16* w2 = (const u16*)d_in[12]; const u16* gw = (const u16*)d_in[13];
    unsigned char* ws = (unsigned char*)d_ws;
    (void)hipFuncSetAttribute((const void*)k1_gemm, hipFuncAttributeMaxDynamicSharedMemorySize, K1_LDS);
    (void)hipFuncSetAttribute((const void*)k2_gemm, hipFuncAttributeMaxDynamicSharedMemorySize, K2_LDS);
    k0_prep<<<256, 512, 0, stream>>>(x, gw, lre, lim, ldt, ws);
    k1_gemm<<<256, 512, K1_LDS, stream>>>(x, w1, w3, Bre, Bim, ws);
    k2_gemm<<<256, 512, K2_LDS, stream>>>(x, w2, Cre, Cim, dsk, ws, (float*)d_out);
}
```

```cpp
#include <hip/hip_runtime.h>
#include <hip/hip_bf16.h>
#include <stdint.h>

#define LAS __attribute__((address_space(3)))
#define GAS __attribute__((address_space(1)))
typedef unsigned short u16;
typedef unsigned long long u64;
typedef short bf16x8 __attribute__((ext_vector_type(8)));
typedef short s16x4 __attribute__((ext_vector_type(4)));
typedef float f32x4 __attribute__((ext_vector_type(4)));
typedef float f32x2 __attribute__((ext_vector_type(2)));
typedef unsigned u32x4 __attribute__((ext_vector_type(4)));
typedef unsigned u32x2 __attribute__((ext_vector_type(2)));

namespace {
constexpr int S_ = 2048, D_ = 1024, P_ = 1024, F_ = 3584;
constexpr int KS2 = 88;
constexpr size_t WS_GRAN = 0;
constexpr size_t WS_FLAG = 131072;
constexpr size_t WS_ZERO_BYTES = 131072 + 16384;
constexpr size_t WS_G = 262144;
constexpr size_t WS_CH = 262144 + 8192;
constexpr size_t WS_A2 = 1u << 20;
constexpr size_t WS_XB = 32u << 20;
constexpr unsigned TAG = 0x5A5A0001u;
constexpr unsigned SENT = 0xFFFFFFFFu;

__device__ __forceinline__ float bf2f(u16 v) { return __uint_as_float(((unsigned)v) << 16); }
__device__ __forceinline__ unsigned pk_bf16(float lo, float hi) {
    unsigned r; asm("v_cvt_pk_bf16_f32 %0, %1, %2" : "=v"(r) : "v"(lo), "v"(hi)); return r; }
__device__ __forceinline__ f32x2 cmul(f32x2 a, f32x2 b) { return (f32x2){a.x * b.x - a.y * b.y, a.x * b.y + a.y * b.x}; }
__device__ __forceinline__ f32x2 cfma(f32x2 a, f32x2 b, f32x2 c) { return (f32x2){a.x * b.x - a.y * b.y + c.x, a.x * b.y + a.y * b.x + c.y}; }

#define STAMP(i)
#define STAMP_DECL
#define STAMP_OUT(kern)
#define WAIT_V(n) asm volatile("s_waitcnt vmcnt(" #n ")" ::: "memory")
#define WAIT_L(n) asm volatile("s_waitcnt lgkmcnt(" #n ")" ::: "memory")
#define BAR __builtin_amdgcn_s_barrier()
#define SCHED __builtin_amdgcn_sched_barrier(0)
#define DSR128(dst, addr, off) asm volatile("ds_read_b128 %0, %1 offset:%2" : "=v"(dst) : "v"(addr), "i"(off))
#define DSRTR(dst, addr, off) asm volatile("ds_read_b64_tr_b16 %0, %1 offset:%2" : "=v"(dst) : "v"(addr), "i"(off))
#define CAT(lo, hi) __builtin_shufflevector(lo, hi, 0, 1, 2, 3, 4, 5, 6, 7)
#define MKRS(p, bytes) __builtin_amdgcn_make_buffer_rsrc((void*)(p), 0, (int)(bytes), 0x00020000)
#define BLDS(rs, dst, voff, soff) __builtin_amdgcn_raw_ptr_buffer_load_lds((rs), (LAS void*)(dst), 16, (int)(voff), (int)(soff), 0, 0)

__global__ __launch_bounds__(512) void k0_prep(const u16* __restrict__ x, const u16* __restrict__ gw, const u16* __restrict__ lre,
                                               const u16* __restrict__ lim, const u16* __restrict__ ldt, const u16* __restrict__ w1,
                                               const u16* __restrict__ w3, const u16* __restrict__ Bre, const u16* __restrict__ Bim,
                                               unsigned char* __restrict__ ws) {
    const int tid = threadIdx.x, wid = tid >> 6, lane = tid & 63, bid = blockIdx.x;
    unsigned pfv = 0;
    if (tid < 144) {
        const int L = bid * 144 + tid;
        const char* p;
        if (L < 28672) { const int j = L < 14336 ? L : L - 14336; p = (const char*)(L < 14336 ? w1 : w3) + (size_t)j * 128; }
        else { const int j = L - 28672, r = (j >> 2) & 1023; p = (const char*)(j < 4096 ? Bre : Bim) + (size_t)r * 2048 + (j & 3) * 128; }
        pfv = *(const unsigned*)p;
    }
    const int row = bid * 8 + wid;
    const u32x4* xr = (const u32x4*)(x + (size_t)row * D_);
    const u32x4* gr = (const u32x4*)gw;
    float s = 0.f;
#pragma unroll
    for (int i = 0; i < 2; ++i) {
        const u32x4 a = xr[lane + 64 * i], b = gr[lane + 64 * i];
#pragma unroll
        for (int j = 0; j < 4; ++j) {
            s += __uint_as_float(a[j] << 16) * __uint_as_float(b[j] << 16);
            s += __uint_as_float(a[j] & 0xffff0000u) * __uint_as_float(b[j] & 0xffff0000u);
        }
    }
#pragma unroll
    for (int o = 32; o >= 1; o >>= 1) s += __shfl_xor(s, o);
    if (lane == 0) ((float*)(ws + WS_G))[row] = 1.f / (1.f + __expf(-s));
    if (tid < 36) ((u32x4*)(ws + (size_t)bid * 576))[tid] = (u32x4){0u, 0u, 0u, 0u};
    asm volatile("" :: "v"(pfv));
    if (bid < 2) {
        const int p = bid * 512 + tid;
        const float lr = bf2f(lre[p]), li = bf2f(lim[p]), dt = expf(bf2f(ldt[p]));
        const float er = expf(dt * lr); float sn, cs; sincosf(dt * li, &sn, &cs);
        const float br = er * cs, bi = er * sn;
        const float nr = br - 1.f, ni = bi, den = lr * lr + li * li;
        ((f32x4*)(ws + WS_CH))[p] = (f32x4){br, bi, (nr * lr + ni * li) / den, (ni * lr - nr * li) / den};
    }
}

constexpr int K1_A = 0, K1_W1 = 32768, K1_W3 = 32768 + 14336 + 128, K1_BS = K1_W3 + 14336, K1_BUF = K1_BS + 8192;
constexpr int K1_X0 = 2 * K1_BUF;
constexpr int K1_G = K1_X0, K1_CH = K1_X0 + 1024, K1_AGG = K1_X0 + 2048, K1_XC = K1_X0 + 3072, K1_PF = K1_X0 + 3328, K1_LDS = K1_X0 + 3840;
constexpr int RS = 368;

__device__ __forceinline__ size_t a2_addr(int R, int cf) {
    const int rt = R >> 7, rr = R & 127, s = cf >> 3, c = cf & 7;
    return ((size_t)(rt * KS2 + s) * 128 + rr) * 128 + ((c ^ ((rr >> 1) & 7)) << 4);
}

__global__ __launch_bounds__(512, 2) void k1_gemm(const u16* __restrict__ x, const u16* __restrict__ w1, const u16* __restrict__ w3,
                                                  const u16* __restrict__ Bre, const u16* __restrict__ Bim, const u16* __restrict__ w2,
                                                  const u16* __restrict__ Cre, const u16* __restrict__ Cim, unsigned char* __restrict__ ws) {
    extern __shared__ __attribute__((aligned(16))) unsigned char smem[];
    LAS unsigned char* lds = (LAS unsigned char*)smem;
    const int tid = threadIdx.x, wid = __builtin_amdgcn_readfirstlane(tid >> 6), lane = tid & 63;
    const int wm = wid & 3, wn = wid >> 2, fr = lane & 15, fq = lane >> 4;
    const int bid = blockIdx.x, xcd = bid & 7, rr_ = bid >> 3, cg = xcd * 4 + (rr_ & 3), pm = rr_ >> 2;

    STAMP_DECL; STAMP(0);
    const auto rsX = MKRS(x, S_ * D_ * 2);
    const int sA = pm * 524288 + wid * 65536;
    unsigned voffA[2];
#pragma unroll
    for (int i = 0; i < 2; ++i) voffA[i] = (unsigned)((lane >> 3) * 2048 + (((lane & 7) ^ ((i * 4 + (lane >> 4)) & 7)) << 4));
    unsigned voffF[4]; int ldsF[4];
    const auto rsF0 = MKRS(w1, D_ * F_ * 2);
    const auto rsF1 = MKRS(wid + 8 < 14 ? w1 : w3, D_ * F_ * 2);
    const auto rsF2 = MKRS(w3, D_ * F_ * 2);
#pragma unroll
    for (int jj = 0; jj < 4; ++jj) {
        int j = wid + 8 * jj; if (j >= 28) j = 16 + wid;
        const int mat = j / 14, v = j % 14, idx = 64 * v + lane, k = idx / 14, pc = idx % 14;
        voffF[jj] = (unsigned)(k * 7168 + ((pc ^ ((k >> 3) & 1)) << 4));
        ldsF[jj] = (mat ? K1_W3 : K1_W1) + v * 1024;
    }
    const unsigned voffS = (unsigned)((8 * (wid & 1) + (lane >> 3)) * 2048 + (((lane & 7) ^ ((4 * (wid & 1) + (lane >> 4)) & 7)) << 4));
    const auto rsS = MKRS(((wid >> 1) & 1) ? Bim : Bre, P_ * D_ * 2);
    const int sS = (cg * 32 + 16 * (wid >> 2)) * 2048;

#define STAGE_A(par, t) do { _Pragma("unroll") for (int _i = 0; _i < 4; ++_i) \
        BLDS(rsX, lds + (par) * K1_BUF + K1_A + (wid * 4 + _i) * 1024, voffA[_i & 1], sA + (t) * 128 + _i * 16384); } while (0)
#define STAGE_B(par, t) do { const int _sf = cg * 224 + (t) * (64 * 7168); \
        BLDS(rsF0, lds + (par) * K1_BUF + ldsF[0], voffF[0], _sf); \
        BLDS(rsF1, lds + (par) * K1_BUF + ldsF[1], voffF[1], _sf); \
        BLDS(rsF2, lds + (par) * K1_BUF + ldsF[2], voffF[2], _sf); \
        if (wn == 0) BLDS(rsF2, lds + (par) * K1_BUF + ldsF[3], voffF[3], _sf); \
        BLDS(rsS, lds + (par) * K1_BUF + K1_BS + wid * 1024, voffS, sS + (t) * 128); } while (0)

    const u16* pfp; int pfs, pfk, pfb;
    if (bid < 32) { pfp = x; pfs = 11; pfk = 128; pfb = bid * 131072; }
    else if (bid < 144) { pfp = bid < 88 ? w1 : w3; pfs = 7; pfk = 64 * 7168; pfb = (bid < 88 ? bid - 32 : bid - 88) * 8192; }
    else { pfp = bid < 160 ? Bre : Bim; pfs = 11; pfk = 128; pfb = ((bid - 144) & 15) * 131072; }
#define PREFETCH(t) do { int _z = threadIdx.x; asm volatile("" : "+v"(_z)); _z &= 63; \
        if (wid == 4) { const auto _r = MKRS(pfp, D_ * F_ * 2); \
            __builtin_amdgcn_raw_ptr_buffer_load_lds(_r, (LAS void*)(lds + K1_PF), 4, _z << pfs, pfb + ((t) + 4) * pfk, 0, 0); \
        } else { const int _u = bid + 256 * (t); const auto _r = MKRS(_u < 896 ? w2 : (_u < 1152 ? Cre : Cim), F_ * D_ * 2); \
            __builtin_amdgcn_raw_ptr_buffer_load_lds(_r, (LAS void*)(lds + K1_PF + 256), 4, _z << 7, (_u < 896 ? _u : (_u - 896) & 255) * 8192, 0, 0); } } while (0)
    const unsigned lb = (unsigned)(size_t)lds;
    const int sw = fq ^ (fr >> 1);
    const int tq = (lane & 15) >> 2, tp = lane & 3;
    const int tbase = K1_W1 + (8 * fq + tq) * 224 + (tp & 1) * (K1_W3 - K1_W1) + 8 * (tp >> 1);
    unsigned aB[2][2], sB[2][2], tB[2][2];
#pragma unroll
    for (int p = 0; p < 2; ++p) {
        aB[p][0] = lb + p * K1_BUF + K1_A + (wm * 64 + fr) * 128 + (sw << 4); aB[p][1] = lb + p * K1_BUF + K1_A + (wm * 64 + fr) * 128 + ((sw ^ 4) << 4);
        sB[p][0] = lb + p * K1_BUF + K1_BS + fr * 128 + (sw << 4); sB[p][1] = lb + p * K1_BUF + K1_BS + fr * 128 + ((sw ^ 4) << 4);
        tB[p][0] = lb + p * K1_BUF + tbase + ((fq & 1) << 4); tB[p][1] = lb + p * K1_BUF + tbase + (((fq & 1) ^ 1) << 4);
    }

    f32x4 acc[4][9];
#pragma unroll
    for (int m = 0; m < 4; ++m)
#pragma unroll
        for (int n = 0; n < 9; ++n) acc[m][n] = (f32x4){0.f, 0.f, 0.f, 0.f};
    bf16x8 Af[4][2], Bs[3][2]; s16x4 Bl[3][2], Bh[3][2];

#define LDA(par) do { _Pragma("unroll") for (int _m = 0; _m < 4; ++_m) { \
        DSR128(Af[_m][0], aB[par][0], _m * 2048); DSR128(Af[_m][1], aB[par][1], _m * 2048); } } while (0)
#define LDF(par, slot, i) do { \
        DSRTR(Bl[slot][0], tB[par][(i) & 1], ((i) >> 1) * 32); DSRTR(Bh[slot][0], tB[par][(i) & 1], 4 * 224 + ((i) >> 1) * 32); \
        DSRTR(Bl[slot][1], tB[par][(i) & 1], 32 * 224 + ((i) >> 1) * 32); DSRTR(Bh[slot][1], tB[par][(i) & 1], 36 * 224 + ((i) >> 1) * 32); } while (0)
#define LDS_(par, slot, tau) do { DSR128(Bs[slot][0], sB[par][0], (tau) * 2048); DSR128(Bs[slot][1], sB[par][1], (tau) * 2048); } while (0)
#define WA asm volatile("" : "+v"(Af[0][0]), "+v"(Af[0][1]), "+v"(Af[1][0]), "+v"(Af[1][1]), "+v"(Af[2][0]), "+v"(Af[2][1]), "+v"(Af[3][0]), "+v"(Af[3][1]))
#define WF(s) asm volatile("" : "+v"(Bl[s][0]), "+v"(Bh[s][0]), "+v"(Bl[s][1]), "+v"(Bh[s][1]))
#define WS_(s) asm volatile("" : "+v"(Bs[s][0]), "+v"(Bs[s][1]))
#define MMA3(n0, NS) do { __builtin_amdgcn_s_setprio(1); \
        _Pragma("unroll") for (int _k = 0; _k < 2; ++_k) _Pragma("unroll") for (int _m = 0; _m < 4; ++_m) _Pragma("unroll") for (int _n = 0; _n < 3; ++_n) { \
            if ((n0) + _n >= (NS)) acc[_m][(n0) + _n] = __builtin_amdgcn_mfma_f32_16x16x32_bf16(Af[_m][_k], Bs[_n][_k], acc[_m][(n0) + _n], 0, 0, 0); \
            else acc[_m][(n0) + _n] = __builtin_amdgcn_mfma_f32_16x16x32_bf16(CAT(Bl[_n][_k], Bh[_n][_k]), Af[_m][_k], acc[_m][(n0) + _n], 0, 0, 0); } \
        __builtin_amdgcn_s_setprio(0); } while (0)

    { const auto rsW = MKRS(ws, 1u << 20);
      if (wid == 0) BLDS(rsW, lds + K1_G, lane * 16, WS_G + pm * 1024);
      if (wid == 1) BLDS(rsW, lds + K1_CH, (lane & 31) * 16, WS_CH + cg * 512); }
    { u32x4* xb = (u32x4*)(ws + WS_XB + (size_t)bid * 32768) + tid;
#pragma unroll
      for (int i = 0; i < 4; ++i) xb[i * 512] = (u32x4){SENT, SENT, SENT, SENT}; }
    STAGE_A(0, 0); STAGE_B(0, 0); STAGE_A(1, 1);
    WAIT_V(4); BAR;
    STAMP(1);
    if (wn == 0) {
#define STEP0(par, t) do { \
        LDA(par); LDF(par, 0, 0); LDF(par, 1, 1); LDF(par, 2, 2); \
        BAR; WAIT_L(0); WA; WF(0); WF(1); WF(2); SCHED; MMA3(0, 7); BAR; SCHED; \
        LDF(par, 0, 3); LDF(par, 1, 4); LDF(par, 2, 5); if ((t) + 1 < 16) STAGE_B((par) ^ 1, (t) + 1); \
        BAR; WAIT_L(0); WF(0); WF(1); WF(2); SCHED; MMA3(3, 7); BAR; SCHED; \
        LDF(par, 0, 6); LDS_(par, 1, 0); LDS_(par, 2, 1); if ((t) + 2 < 16) STAGE_A(par, (t) + 2); \
        BAR; WAIT_L(0); WF(0); WS_(1); WS_(2); SCHED; MMA3(6, 7); if ((t) + 2 < 16) WAIT_V(4); else WAIT_V(0); BAR; SCHED; } while (0)
        for (int t = 0; t < 16; t += 2) { STEP0(0, t); STEP0(1, t + 1); }
    } else {
        BAR;
#define STEP1(par, t, LASTBAR) do { \
        LDA(par); LDF(par, 0, 7); LDF(par, 1, 8); LDF(par, 2, 9); if ((t) + 1 < 16) STAGE_B((par) ^ 1, (t) + 1); \
        BAR; WAIT_L(0); WA; WF(0); WF(1); WF(2); SCHED; MMA3(0, 7); BAR; SCHED; \
        LDF(par, 0, 10); LDF(par, 1, 11); LDF(par, 2, 12); if ((t) + 2 < 16) STAGE_A(par, (t) + 2); \
        BAR; WAIT_L(0); WF(0); WF(1); WF(2); SCHED; MMA3(3, 7); BAR; SCHED; \
        LDF(par, 0, 13); LDS_(par, 1, 2); LDS_(par, 2, 3); \
        if ((wid == 4 && (t) < 12 && bid < 176) || (wid == 5 && bid + 256 * (t) < 1408)) { PREFETCH(t); WAIT_V(5); } else if ((t) + 2 < 16) WAIT_V(4); else WAIT_V(0); \
        BAR; WAIT_L(0); WF(0); WS_(1); WS_(2); SCHED; MMA3(6, 7); if (LASTBAR) BAR; SCHED; } while (0)
        for (int t = 0; t < 16; t += 2) { STEP1(0, t, true); STEP1(1, t + 1, t + 1 < 15); }
    }

    STAMP(2);
    {
    int tz = threadIdx.x; asm volatile("" : "+v"(tz));
    const int tid = tz, lane = tz & 63, fr = lane & 15, fq = lane >> 4;
    const LAS float* sg = (const LAS float*)(lds + K1_G);
    unsigned char* A2 = ws + WS_A2;
#define FFN_EPI(n, i) do { _Pragma("unroll") for (int _m = 0; _m < 4; ++_m) { \
        const f32x4 _v = acc[_m][n]; \
        const auto _s0 = __builtin_amdgcn_permlane16_swap(__float_as_uint(_v[0]), __float_as_uint(_v[2]), false, false); \
        const auto _s1 = __builtin_amdgcn_permlane16_swap(__float_as_uint(_v[1]), __float_as_uint(_v[3]), false, false); \
        const f32x2 _g = (f32x2){__uint_as_float(_s0[0]), __uint_as_float(_s1[0])}, _u = (f32x2){__uint_as_float(_s0[1]), __uint_as_float(_s1[1])}; \
        const f32x2 _t = _g * (-1.44269504089f); \
        const f32x2 _d = (f32x2){__builtin_amdgcn_exp2f(_t.x), __builtin_amdgcn_exp2f(_t.y)} + 1.0f; \
        const f32x2 _r = (f32x2){__builtin_amdgcn_rcpf(_d.x), __builtin_amdgcn_rcpf(_d.y)}; \
        const f32x2 _h = (_g * _u) * (_r * omg[_m]); \
        *(LAS unsigned*)(lds + (wm * 64 + _m * 16 + fr) * RS + (i) * 16 + fq * 4) = pk_bf16(_h.x, _h.y); } } while (0)

    const LAS f32x4* sch = (const LAS f32x4*)(lds + K1_CH);
    LAS f32x2* sagg = (LAS f32x2*)(lds + K1_AGG);
    f32x2 lam, l16, l64, lq, cf, ex[4], T[4];
    {
        const f32x4 c = sch[wn * 16 + fr];
        lam = (f32x2){c[0], c[1]}; cf = (f32x2){c[2], c[3]};
        const f32x2 l2 = cmul(lam, lam), l4 = cmul(l2, l2), l8 = cmul(l4, l4); l16 = cmul(l8, l8);
        const f32x2 l32 = cmul(l16, l16); l64 = cmul(l32, l32);
        const f32x2 l12 = cmul(l4, l8);
        lq = fq == 0 ? (f32x2){1.f, 0.f} : (fq == 1 ? l4 : (fq == 2 ? l8 : l12));
        f32x2 W = (f32x2){0.f, 0.f};
#pragma unroll
        for (int m = 0; m < 4; ++m) {
            f32x2 a = (f32x2){0.f, 0.f};
#pragma unroll
            for (int j = 0; j < 4; ++j) a = cfma(lam, a, cmul(cf, (f32x2){acc[m][7][j], acc[m][8][j]}));
            f32x2 x0, x1, x2, x3;
            { const auto p = __builtin_amdgcn_permlane16_swap(__float_as_uint(a.x), __float_as_uint(a.x), false, false);
              const auto q = __builtin_amdgcn_permlane32_swap(p[0], p[0], false, false), r = __builtin_amdgcn_permlane32_swap(p[1], p[1], false, false);
              x0.x = __uint_as_float(q[0]); x2.x = __uint_as_float(q[1]); x1.x = __uint_as_float(r[0]); x3.x = __uint_as_float(r[1]); }
            { const auto p = __builtin_amdgcn_permlane16_swap(__float_as_uint(a.y), __float_as_uint(a.y), false, false);
              const auto q = __builtin_amdgcn_permlane32_swap(p[0], p[0], false, false), r = __builtin_amdgcn_permlane32_swap(p[1], p[1], false, false);
              x0.y = __uint_as_float(q[0]); x2.y = __uint_as_float(q[1]); x1.y = __uint_as_float(r[0]); x3.y = __uint_as_float(r[1]); }
            const f32x2 e2 = cfma(l4, x0, x1), e3 = cfma(l4, e2, x2);
            T[m] = cfma(l4, e3, x3);
            ex[m] = fq == 0 ? (f32x2){0.f, 0.f} : (fq == 1 ? x0 : (fq == 2 ? e2 : e3));
            W = cfma(l16, W, T[m]);
        }
        if (fq == 0) sagg[wm * 32 + wn * 16 + fr] = W;
    }
    STAMP(3); WAIT_L(0); BAR;
    STAMP(4);
    GAS u64* gran = (GAS u64*)(ws + WS_GRAN) + (size_t)cg * 8 * 64;
    f32x2 l256 = (f32x2){0.f, 0.f};
    if (wid == 0) {
        const int ch = lane & 31, part = lane >> 5;
        const f32x4 c = sch[ch];
        f32x2 p = (f32x2){c[0], c[1]};
#pragma unroll
        for (int i = 0; i < 6; ++i) p = cmul(p, p);
        f32x2 a = sagg[ch]; a = cfma(p, a, sagg[32 + ch]); a = cfma(p, a, sagg[64 + ch]); a = cfma(p, a, sagg[96 + ch]);
        l256 = cmul(p, p); l256 = cmul(l256, l256);
        if (pm < 7) __hip_atomic_store(gran + pm * 64 + ch * 2 + part, ((u64)TAG << 32) | __float_as_uint(part ? a.y : a.x), __ATOMIC_RELAXED, __HIP_MEMORY_SCOPE_AGENT);
    }
    {
        float omg[4];
#pragma unroll
        for (int m = 0; m < 4; ++m) omg[m] = 1.f - sg[wm * 64 + m * 16 + fr];
#pragma unroll
        for (int n = 0; n < 7; ++n) FFN_EPI(n, wn * 7 + n);
    }
    if (wid == 0) {
        const int ch = lane & 31, part = lane >> 5;
        f32x2 X = (f32x2){0.f, 0.f};
        if (pm > 0) {
            u64 v[7];
            for (unsigned spins = 0;; ++spins) {
                bool ok = true;
#pragma unroll
                for (int j = 0; j < 7; ++j) { const int jj = j < pm ? j : pm - 1;
                    v[j] = __hip_atomic_load(gran + jj * 64 + ch * 2 + part, __ATOMIC_RELAXED, __HIP_MEMORY_SCOPE_AGENT); ok &= (unsigned)(v[j] >> 32) == TAG; }
                if (__all(ok) || spins > (1u << 20)) break;
                __builtin_amdgcn_s_sleep(1);
            }
#pragma unroll
            for (int j = 0; j < 7; ++j) { const float mine = __uint_as_float((unsigned)v[j]), other = __shfl_xor(mine, 32);
                const f32x2 aj = part ? (f32x2){other, mine} : (f32x2){mine, other};
                if (j < pm) X = cfma(l256, X, aj); }
        }
        if (lane < 32) ((LAS f32x2*)(lds + K1_XC))[ch] = X;
    }
    STAMP(5); WAIT_L(0); BAR;
    STAMP(6);
    {
        f32x2 c = ((const LAS f32x2*)(lds + K1_XC))[wn * 16 + fr];
        for (int w = 0; w < wm; ++w) c = cfma(l64, c, sagg[w * 32 + wn * 16 + fr]);
#pragma unroll
        for (int m = 0; m < 4; ++m) {
            f32x2 h = cfma(lq, c, ex[m]);
            const f32x4 g4 = *(const LAS f32x4*)(sg + wm * 64 + m * 16 + fq * 4);
#pragma unroll
            for (int j = 0; j < 4; ++j) {
                h = cfma(lam, h, cmul(cf, (f32x2){acc[m][7][j], acc[m][8][j]}));
                const float s2 = 2.f * g4[j];
                const unsigned pk = pk_bf16(s2 * h.x, -s2 * h.y);
                LAS unsigned char* dst = lds + (wm * 64 + m * 16 + fq * 4 + j) * RS + 224 + (wn * 16 + fr) * 2;
                *(LAS u16*)dst = (u16)(pk & 0xffffu);
                *(LAS u16*)(dst + 64) = (u16)(pk >> 16);
            }
            c = cfma(l16, c, T[m]);
        }
    }
#pragma unroll
    for (int it = 0; it < 7; ++it) { const int idx = it * 512 + tid, row = idx / 14, j = idx % 14;
        const u32x4 v = *(const LAS u32x4*)(lds + row * RS + j * 16);
        *(u32x4*)(A2 + a2_addr(pm * 256 + row, cg * 14 + j)) = v; }
    STAMP(7); WAIT_L(0); BAR;
    STAMP(8);
#pragma unroll
    for (int it = 0; it < 4; ++it) { const int idx = it * 512 + tid, row = idx >> 3, j = idx & 7;
        const u32x4 v = *(const LAS u32x4*)(lds + row * RS + 224 + j * 16);
        *(u32x4*)(A2 + a2_addr(pm * 256 + row, 448 + (j >> 2) * 128 + cg * 4 + (j & 3))) = v; }
    STAMP(9);
    STAMP(10); STAMP_OUT(0);
    }
}

constexpr int K2_ST = 32768, K2_NST = 5, K2_LDS = K2_NST * K2_ST;

__global__ __launch_bounds__(512, 2) void k2_gemm(const u16* __restrict__ x, const u16* __restrict__ w2, const u16* __restrict__ Cre,
                                                  const u16* __restrict__ Cim, const u16* __restrict__ dsk, unsigned char* __restrict__ ws,
                                                  float* __restrict__ out) {
    extern __shared__ __attribute__((aligned(16))) unsigned char smem[];
    LAS unsigned char* lds = (LAS unsigned char*)smem;
    const int tid = threadIdx.x, wid = __builtin_amdgcn_readfirstlane(tid >> 6), lane = tid & 63;
    const int grp = wid >> 2, wm = (wid >> 1) & 1, wn = wid & 1, fr = lane & 15, fq = lane >> 4;
    const int bid = blockIdx.x, xcd = bid & 7, r_ = bid >> 3, kh = xcd >> 2, sub = xcd & 3;
    const int rt = (sub >> 1) * 8 + (r_ >> 2), ct = (sub & 1) * 4 + (r_ & 3);
    const int s0 = kh * 44;
    constexpr int NS = 44;

    STAMP_DECL; STAMP(0);
    const int w4 = wid & 3;
    const auto rsA = MKRS(ws + WS_A2, S_ * 5632 * 2);
    const int sA = (rt * KS2 + s0) * 16384 + w4 * 4096;
    const unsigned voffL = lane * 16;
    unsigned voffW[2], voffC[2];
#pragma unroll
    for (int i = 0; i < 2; ++i) {
        voffW[i] = (unsigned)((lane >> 4) * 2048 + ((((lane & 15) >> 1) ^ ((lane >> 4) | (i << 2))) << 5) + ((lane & 1) << 4));
        voffC[i] = (unsigned)((lane >> 3) * 2048 + (((lane & 7) ^ ((i * 4 + (lane >> 4)) & 7)) << 4)); }
    const auto rsW = MKRS(w2, F_ * D_ * 2);
    const int sW = w4 * 16 * 2048 + ct * 256 + s0 * (64 * 2048);
    const auto rsCr = MKRS(Cre, D_ * P_ * 2); const auto rsCi = MKRS(Cim, D_ * P_ * 2);
    const int sC = (ct * 128 + w4 * 32) * 2048;
#define BLDS2 BLDS
#define STAGE2(sl, so) do { LAS unsigned char* _d = lds + (so) + w4 * 4096; \
        _Pragma("unroll") for (int _i = 0; _i < 4; ++_i) BLDS2(rsA, _d + _i * 1024, voffL, sA + (sl) * 16384 + _i * 1024); \
        if (s0 + (sl) < 56) { _Pragma("unroll") for (int _i = 0; _i < 4; ++_i) BLDS2(rsW, _d + 16384 + _i * 1024, voffW[_i >> 1], sW + (sl) * (64 * 2048) + _i * 8192); } \
        else { const auto _r = (sl) < 28 ? rsCr : rsCi; \
               _Pragma("unroll") for (int _i = 0; _i < 4; ++_i) BLDS2(_r, _d + 16384 + _i * 1024, voffC[_i & 1], sC + (((sl) - 12) & 15) * 128 + _i * 16384); } } while (0)

    const unsigned lb = (unsigned)(size_t)lds;
    const int sw = (fq ^ (fr >> 1)) << 4;
    const unsigned aoff0 = lb + (wm * 64 + fr) * 128 + sw, aoff1 = lb + (wm * 64 + fr) * 128 + (sw ^ 64);
    const unsigned coff0 = lb + 16384 + (wn * 64 + fr) * 128 + sw, coff1 = lb + 16384 + (wn * 64 + fr) * 128 + (sw ^ 64);
    const int tq = (lane & 15) >> 2, tp = lane & 3, fl = tq | ((fq & 1) << 2);
    unsigned toff[4];
#pragma unroll
    for (int n = 0; n < 4; ++n) toff[n] = lb + 16384 + (8 * fq + tq) * 256 + (((wn * 4 + n) ^ fl) << 5) + 8 * tp;

    f32x4 acc[4][4];
#pragma unroll
    for (int m = 0; m < 4; ++m)
#pragma unroll
        for (int n = 0; n < 4; ++n) acc[m][n] = (f32x4){0.f, 0.f, 0.f, 0.f};
    bf16x8 Af[4][2], Bf[4][2]; s16x4 Bl[4][2], Bh[4][2];

    if (grp) { STAGE2(0, 0); STAGE2(2, 2 * K2_ST); WAIT_V(8); } else STAGE2(1, K2_ST);
    BAR;
    if (grp) BAR;
    STAMP(1);
    int sl = grp, rdo = grp * K2_ST, sto = (grp + 3) * K2_ST;
#define K2_ADV do { sl += 2; rdo += 2 * K2_ST; if (rdo >= K2_LDS) rdo -= K2_LDS; sto += 2 * K2_ST; if (sto >= K2_LDS) sto -= K2_LDS; } while (0)
#define K2_MMA(BEXPR) do { __builtin_amdgcn_s_setprio(1); \
        _Pragma("unroll") for (int k = 0; k < 2; ++k) _Pragma("unroll") for (int m = 0; m < 4; ++m) _Pragma("unroll") for (int n = 0; n < 4; ++n) \
            acc[m][n] = __builtin_amdgcn_mfma_f32_16x16x32_bf16(BEXPR, Af[m][k], acc[m][n], 0, 0, 0); \
        __builtin_amdgcn_s_setprio(0); } while (0)
#define K2_DMA(TY, i) do { if ((TY) != 2) { LAS unsigned char* _d = lds + sto + w4 * 4096; \
        if ((i) < 4) BLDS2(rsA, _d + (i) * 1024, voffL, sA + (sl + 3) * 16384 + (i) * 1024); \
        else if ((TY) == 0) BLDS2(rsW, _d + 16384 + ((i) - 4) * 1024, voffW[((i) - 4) >> 1], sW + (sl + 3) * (64 * 2048) + ((i) - 4) * 8192); \
        else BLDS2(rsC, _d + 16384 + ((i) - 4) * 1024, voffC[(i) & 1], sC + ((sl + 3 - 12) & 15) * 128 + ((i) - 4) * 16384); } } while (0)
#define K2_STEP(RT, TY) do { \
        const unsigned a0 = aoff0 + rdo, a1 = aoff1 + rdo; const auto rsC = sl + 3 < 28 ? rsCr : rsCi; \
        if ((RT) == 0) { \
            _Pragma("unroll") for (int n = 0; n < 4; ++n) { const unsigned t = toff[n] + rdo; \
                K2_DMA(TY, 2 * n); DSRTR(Bl[n][0], t, 0); DSRTR(Bh[n][0], t, 1024); DSR128(Af[n][0], a0, n * 2048); \
                K2_DMA(TY, 2 * n + 1); DSRTR(Bl[n][1], t, 8192); DSRTR(Bh[n][1], t, 9216); DSR128(Af[n][1], a1, n * 2048); } \
        } else { const unsigned c0 = coff0 + rdo, c1 = coff1 + rdo; \
            _Pragma("unroll") for (int n = 0; n < 4; ++n) { \
                K2_DMA(TY, 2 * n); DSR128(Bf[n][0], c0, n * 2048); DSR128(Af[n][0], a0, n * 2048); \
                K2_DMA(TY, 2 * n + 1); DSR128(Bf[n][1], c1, n * 2048); DSR128(Af[n][1], a1, n * 2048); } } \
        if ((TY) != 2) WAIT_V(8); else WAIT_V(0); \
        BAR; WAIT_L(0); \
        asm volatile("" : "+v"(Af[0][0]), "+v"(Af[0][1]), "+v"(Af[1][0]), "+v"(Af[1][1]), "+v"(Af[2][0]), "+v"(Af[2][1]), "+v"(Af[3][0]), "+v"(Af[3][1])); \
        if ((RT) == 0) { \
            asm volatile("" : "+v"(Bl[0][0]), "+v"(Bh[0][0]), "+v"(Bl[1][0]), "+v"(Bh[1][0]), "+v"(Bl[2][0]), "+v"(Bh[2][0]), "+v"(Bl[3][0]), "+v"(Bh[3][0]), \
                              "+v"(Bl[0][1]), "+v"(Bh[0][1]), "+v"(Bl[1][1]), "+v"(Bh[1][1]), "+v"(Bl[2][1]), "+v"(Bh[2][1]), "+v"(Bl[3][1]), "+v"(Bh[3][1])); \
            SCHED; K2_MMA(CAT(Bl[n][k], Bh[n][k])); \
        } else { \
            asm volatile("" : "+v"(Bf[0][0]), "+v"(Bf[0][1]), "+v"(Bf[1][0]), "+v"(Bf[1][1]), "+v"(Bf[2][0]), "+v"(Bf[2][1]), "+v"(Bf[3][0]), "+v"(Bf[3][1])); \
            SCHED; K2_MMA(Bf[n][k]); } \
        SCHED; K2_ADV; BAR; SCHED; } while (0)
    if (kh == 0) {
        for (; sl < NS - 3;) K2_STEP(0, 0);
        for (; sl < NS;) K2_STEP(0, 2);
    } else {
        for (; sl < 9;) K2_STEP(0, 0);
        for (; sl < 12;) K2_STEP(0, 1);
        for (; sl < NS - 3;) K2_STEP(1, 1);
        for (; sl < NS;) K2_STEP(1, 2);
    }
    STAMP(2);
    if (grp == 0) BAR;
    STAMP(3);
    {
        LAS f32x4* xs = (LAS f32x4*)(lds + ((wid & 3) * 2 + grp) * 8192);
        const LAS f32x4* xr = (const LAS f32x4*)(lds + ((wid & 3) * 2 + (grp ^ 1)) * 8192);
        f32x4 r[2][4];
        if (grp) {
#pragma unroll
            for (int mm = 0; mm < 2; ++mm)
#pragma unroll
                for (int n = 0; n < 4; ++n) { xs[(mm * 4 + n) * 64 + lane] = acc[mm][n]; r[mm][n] = acc[2 + mm][n]; }
        } else {
#pragma unroll
            for (int mm = 0; mm < 2; ++mm)
#pragma unroll
                for (int n = 0; n < 4; ++n) { xs[(mm * 4 + n) * 64 + lane] = acc[2 + mm][n]; r[mm][n] = acc[mm][n]; }
        }
        const int mt = 2 * grp + kh;
        const int row = rt * 128 + wm * 64 + mt * 16 + fr;
        const float g = ((const float*)(ws + WS_G))[row];
        u32x2 xv[4], dv[4];
#pragma unroll
        for (int n = 0; n < 4; ++n) { const int col = ct * 128 + wn * 64 + n * 16 + fq * 4;
            xv[n] = *(const u32x2*)(x + (size_t)row * D_ + col); dv[n] = *(const u32x2*)(dsk + col); }
        WAIT_L(0); BAR;
#pragma unroll
        for (int mm = 0; mm < 2; ++mm)
#pragma unroll
            for (int n = 0; n < 4; ++n) r[mm][n] += xr[(mm * 4 + n) * 64 + lane];
        f32x4 mine[4], give[4];
        if (kh) {
#pragma unroll
            for (int n = 0; n < 4; ++n) { mine[n] = r[1][n]; give[n] = r[0][n]; }
        } else {
#pragma unroll
            for (int n = 0; n < 4; ++n) { mine[n] = r[0][n]; give[n] = r[1][n]; }
        }
        const int pbid = bid ^ 4;
        const auto rs = MKRS(ws + WS_XB, 256 * 32768);
#pragma unroll
        for (int n = 0; n < 4; ++n)
            __builtin_amdgcn_raw_buffer_store_b128(__builtin_bit_cast(u32x4, give[n]), rs, bid * 32768 + wid * 4096 + n * 1024 + lane * 16, 0, 16);
        STAMP(4);
#pragma unroll
        for (int n = 0; n < 4; ++n) {
            mine[n][0] += g * __uint_as_float(xv[n][0] << 16) * __uint_as_float(dv[n][0] << 16);
            mine[n][1] += g * __uint_as_float(xv[n][0] & 0xffff0000u) * __uint_as_float(dv[n][0] & 0xffff0000u);
            mine[n][2] += g * __uint_as_float(xv[n][1] << 16) * __uint_as_float(dv[n][1] << 16);
            mine[n][3] += g * __uint_as_float(xv[n][1] & 0xffff0000u) * __uint_as_float(dv[n][1] & 0xffff0000u); }
        u32x4 pv[4];
        const unsigned char* pp = ws + WS_XB + (size_t)pbid * 32768 + wid * 4096 + lane * 16;
        for (unsigned spins = 0;; ++spins) {
            asm volatile("global_load_dwordx4 %0, %4, off sc1\n\tglobal_load_dwordx4 %1, %4, off offset:1024 sc1\n\t"
                         "global_load_dwordx4 %2, %4, off offset:2048 sc1\n\tglobal_load_dwordx4 %3, %4, off offset:3072 sc1\n\ts_waitcnt vmcnt(0)"
                         : "=&v"(pv[0]), "=&v"(pv[1]), "=&v"(pv[2]), "=&v"(pv[3]) : "v"(pp) : "memory");
            bool ok = true;
#pragma unroll
            for (int n = 0; n < 4; ++n) ok &= (pv[n][0] != SENT) & (pv[n][1] != SENT) & (pv[n][2] != SENT) & (pv[n][3] != SENT);
            if (__all(ok) || spins > (1u << 18)) break;
            __builtin_amdgcn_s_sleep(1);
        }
        STAMP(5);
#pragma unroll
        for (int n = 0; n < 4; ++n)
            *(f32x4*)(out + (size_t)row * D_ + ct * 128 + wn * 64 + n * 16 + fq * 4) = mine[n] + __builtin_bit_cast(f32x4, pv[n]);
        STAMP(6);
        STAMP(7); STAMP_OUT(1);
    }
}
}

extern "C" void kernel_launch(void* const* d_in, const int* in_sizes, int n_in, void* d_out, int out_size, void* d_ws, size_t ws_size,
                              hipStream_t stream) {
    const u16* x = (const u16*)d_in[0]; const u16* lre = (const u16*)d_in[1]; const u16* lim = (const u16*)d_in[2];
    const u16* Bre = (const u16*)d_in[3]; const u16* Bim = (const u16*)d_in[4]; const u16* Cre = (const u16*)d_in[5]; const u16* Cim = (const u16*)d_in[6];
    const u16* ldt = (const u16*)d_in[7]; const u16* dsk = (const u16*)d_in[8];
    const u16* w1 = (const u16*)d_in[10]; const u16* w3 = (const u16*)d_in[11]; const u16* w2 = (const u16*)d_in[12]; const u16* gw = (const u16*)d_in[13];
    unsigned char* ws = (unsigned char*)d_ws;
    (void)hipFuncSetAttribute((const void*)k1_gemm, hipFuncAttributeMaxDynamicSharedMemorySize, K1_LDS);
    (void)hipFuncSetAttribute((const void*)k2_gemm, hipFuncAttributeMaxDynamicSharedMemorySize, K2_LDS);
    k0_prep<<<256, 512, 0, stream>>>(x, gw, lre, lim, ldt, w1, w3, Bre, Bim, ws);
    k1_gemm<<<256, 512, K1_LDS, stream>>>(x, w1, w3, Bre, Bim, w2, Cre, Cim, ws);
    k2_gemm<<<256, 512, K2_LDS, stream>>>(x, w2, Cre, Cim, dsk, ws, (float*)d_out);
}
```

```cpp
#include <hip/hip_runtime.h>
#include <hip/hip_bf16.h>
#include <stdint.h>

#define LAS __attribute__((address_space(3)))
#define GAS __attribute__((address_space(1)))
typedef unsigned short u16;
typedef unsigned long long u64;
typedef short bf16x8 __attribute__((ext_vector_type(8)));
typedef short s16x4 __attribute__((ext_vector_type(4)));
typedef float f32x4 __attribute__((ext_vector_type(4)));
typedef float f32x2 __attribute__((ext_vector_type(2)));
typedef unsigned u32x4 __attribute__((ext_vector_type(4)));
typedef unsigned u32x2 __attribute__((ext_vector_type(2)));

#define A2AUX 0
namespace {
constexpr int S_ = 2048, D_ = 1024, P_ = 1024, F_ = 3584;
constexpr int KS2 = 88;
constexpr size_t WS_GRAN = 0;
constexpr size_t WS_FLAG = 131072;
constexpr size_t WS_ZERO_BYTES = 131072 + 16384;
constexpr size_t WS_G = 262144;
constexpr size_t WS_CH = 262144 + 8192;
constexpr size_t WS_A2 = 1u << 20;
constexpr size_t WS_XB = 32u << 20;
constexpr unsigned TAG = 0x5A5A0001u;
constexpr unsigned SENT = 0xFFFFFFFFu;

__device__ __forceinline__ float bf2f(u16 v) { return __uint_as_float(((unsigned)v) << 16); }
__device__ __forceinline__ unsigned pk_bf16(float lo, float hi) {
    unsigned r; asm("v_cvt_pk_bf16_f32 %0, %1, %2" : "=v"(r) : "v"(lo), "v"(hi)); return r; }
__device__ __forceinline__ f32x2 cmul(f32x2 a, f32x2 b) { return (f32x2){a.x * b.x - a.y * b.y, a.x * b.y + a.y * b.x}; }
__device__ __forceinline__ f32x2 cfma(f32x2 a, f32x2 b, f32x2 c) { return (f32x2){a.x * b.x - a.y * b.y + c.x, a.x * b.y + a.y * b.x + c.y}; }

#define STAMP(i)
#define STAMP_DECL
#define STAMP_OUT(kern)
#define WAIT_V(n) asm volatile("s_waitcnt vmcnt(" #n ")" ::: "memory")
#define WAIT_L(n) asm volatile("s_waitcnt lgkmcnt(" #n ")" ::: "memory")
#define BAR __builtin_amdgcn_s_barrier()
#define SCHED __builtin_amdgcn_sched_barrier(0)
#define DSR128(dst, addr, off) asm volatile("ds_read_b128 %0, %1 offset:%2" : "=v"(dst) : "v"(addr), "i"(off))
#define DSRTR(dst, addr, off) asm volatile("ds_read_b64_tr_b16 %0, %1 offset:%2" : "=v"(dst) : "v"(addr), "i"(off))
#define CAT(lo, hi) __builtin_shufflevector(lo, hi, 0, 1, 2, 3, 4, 5, 6, 7)
#define MKRS(p, bytes) __builtin_amdgcn_make_buffer_rsrc((void*)(p), 0, (int)(bytes), 0x00020000)
#define BLDS(rs, dst, voff, soff) __builtin_amdgcn_raw_ptr_buffer_load_lds((rs), (LAS void*)(dst), 16, (int)(voff), (int)(soff), 0, 0)

__global__ __launch_bounds__(512) void k0_prep(const u16* __restrict__ x, const u16* __restrict__ gw, const u16* __restrict__ lre,
                                               const u16* __restrict__ lim, const u16* __restrict__ ldt, const u16* __restrict__ w1,
                                               const u16* __restrict__ w3, const u16* __restrict__ Bre, const u16* __restrict__ Bim,
                                               unsigned char* __restrict__ ws) {
    const int tid = threadIdx.x, wid = tid >> 6, lane = tid & 63, bid = blockIdx.x;
    unsigned pfv = 0;
    if (tid < 144) {
        const int L = bid * 144 + tid;
        const char* p;
        if (L < 28672) { const int j = L < 14336 ? L : L - 14336; p = (const char*)(L < 14336 ? w1 : w3) + (size_t)j * 128; }
        else { const int j = L - 28672, r = (j >> 2) & 1023; p = (const char*)(j < 4096 ? Bre : Bim) + (size_t)r * 2048 + (j & 3) * 128; }
        pfv = *(const unsigned*)p;
    }
    const int row = bid * 8 + wid;
    const u32x4* xr = (const u32x4*)(x + (size_t)row * D_);
    const u32x4* gr = (const u32x4*)gw;
    float s = 0.f;
#pragma unroll
    for (int i = 0; i < 2; ++i) {
        const u32x4 a = xr[lane + 64 * i], b = gr[lane + 64 * i];
#pragma unroll
        for (int j = 0; j < 4; ++j) {
            s += __uint_as_float(a[j] << 16) * __uint_as_float(b[j] << 16);
            s += __uint_as_float(a[j] & 0xffff0000u) * __uint_as_float(b[j] & 0xffff0000u);
        }
    }
#pragma unroll
    for (int o = 32; o >= 1; o >>= 1) s += __shfl_xor(s, o);
    if (lane == 0) ((float*)(ws + WS_G))[row] = 1.f / (1.f + __expf(-s));
    if (tid < 36) ((u32x4*)(ws + (size_t)bid * 576))[tid] = (u32x4){0u, 0u, 0u, 0u};
    asm volatile("" :: "v"(pfv));
    if (bid < 2) {
        const int p = bid * 512 + tid;
        const float lr = bf2f(lre[p]), li = bf2f(lim[p]), dt = expf(bf2f(ldt[p]));
        const float er = expf(dt * lr); float sn, cs; sincosf(dt * li, &sn, &cs);
        const float br = er * cs, bi = er * sn;
        const float nr = br - 1.f, ni = bi, den = lr * lr + li * li;
        ((f32x4*)(ws + WS_CH))[p] = (f32x4){br, bi, (nr * lr + ni * li) / den, (ni * lr - nr * li) / den};
    }
}

constexpr int K1_A = 0, K1_W1 = 32768, K1_W3 = 32768 + 14336 + 128, K1_BS = K1_W3 + 14336, K1_BUF = K1_BS + 8192;
constexpr int K1_X0 = 2 * K1_BUF;
constexpr int K1_G = K1_X0, K1_CH = K1_X0 + 1024, K1_AGG = K1_X0 + 2048, K1_XC = K1_X0 + 3072, K1_PF = K1_X0 + 3328, K1_LDS = K1_X0 + 3840;
constexpr int RS = 368;

__device__ __forceinline__ size_t a2_addr(int R, int cf) {
    const int rt = R >> 7, rr = R & 127, s = cf >> 3, c = cf & 7;
    return ((size_t)(rt * KS2 + s) * 128 + rr) * 128 + ((c ^ ((rr >> 1) & 7)) << 4);
}

__global__ __launch_bounds__(512, 2) void k1_gemm(const u16* __restrict__ x, const u16* __restrict__ w1, const u16* __restrict__ w3,
                                                  const u16* __restrict__ Bre, const u16* __restrict__ Bim, const u16* __restrict__ w2,
                                                  const u16* __restrict__ Cre, const u16* __restrict__ Cim, unsigned char* __restrict__ ws) {
    extern __shared__ __attribute__((aligned(16))) unsigned char smem[];
    LAS unsigned char* lds = (LAS unsigned char*)smem;
    const int tid = threadIdx.x, wid = __builtin_amdgcn_readfirstlane(tid >> 6), lane = tid & 63;
    const int wm = wid & 3, wn = wid >> 2, fr = lane & 15, fq = lane >> 4;
    const int bid = blockIdx.x, xcd = bid & 7, rr_ = bid >> 3, cg = xcd * 4 + (rr_ & 3), pm = rr_ >> 2;

    STAMP_DECL; STAMP(0);
    const auto rsX = MKRS(x, S_ * D_ * 2);
    const int sA = pm * 524288 + wid * 65536;
    unsigned voffA[2];
#pragma unroll
    for (int i = 0; i < 2; ++i) voffA[i] = (unsigned)((lane >> 3) * 2048 + (((lane & 7) ^ ((i * 4 + (lane >> 4)) & 7)) << 4));
    unsigned voffF[4]; int ldsF[4];
    const auto rsF0 = MKRS(w1, D_ * F_ * 2);
    const auto rsF1 = MKRS(wid + 8 < 14 ? w1 : w3, D_ * F_ * 2);
    const auto rsF2 = MKRS(w3, D_ * F_ * 2);
#pragma unroll
    for (int jj = 0; jj < 4; ++jj) {
        int j = wid + 8 * jj; if (j >= 28) j = 16 + wid;
        const int mat = j / 14, v = j % 14, idx = 64 * v + lane, k = idx / 14, pc = idx % 14;
        voffF[jj] = (unsigned)(k * 7168 + ((pc ^ ((k >> 3) & 1)) << 4));
        ldsF[jj] = (mat ? K1_W3 : K1_W1) + v * 1024;
    }
    const unsigned voffS = (unsigned)((8 * (wid & 1) + (lane >> 3)) * 2048 + (((lane & 7) ^ ((4 * (wid & 1) + (lane >> 4)) & 7)) << 4));
    const auto rsS = MKRS(((wid >> 1) & 1) ? Bim : Bre, P_ * D_ * 2);
    const int sS = (cg * 32 + 16 * (wid >> 2)) * 2048;

#define SA(c, par, t, i) do { if (c) BLDS(rsX, lds + (par) * K1_BUF + K1_A + (wid * 4 + (i)) * 1024, voffA[(i) & 1], sA + (t) * 128 + (i) * 16384); } while (0)
#define SB(c, par, t, j) do { if (c) { const int _sf = cg * 224 + (t) * (64 * 7168); \
        if ((j) == 0) BLDS(rsF0, lds + (par) * K1_BUF + ldsF[0], voffF[0], _sf); \
        if ((j) == 1) BLDS(rsF1, lds + (par) * K1_BUF + ldsF[1], voffF[1], _sf); \
        if ((j) == 2) BLDS(rsF2, lds + (par) * K1_BUF + ldsF[2], voffF[2], _sf); \
        if ((j) == 3) BLDS(rsF2, lds + (par) * K1_BUF + ldsF[3], voffF[3], _sf); \
        if ((j) == 4) BLDS(rsS, lds + (par) * K1_BUF + K1_BS + wid * 1024, voffS, sS + (t) * 128); } } while (0)
#define STAGE_A(par, t) do { _Pragma("unroll") for (int _i = 0; _i < 4; ++_i) \
        BLDS(rsX, lds + (par) * K1_BUF + K1_A + (wid * 4 + _i) * 1024, voffA[_i & 1], sA + (t) * 128 + _i * 16384); } while (0)
#define STAGE_B(par, t) do { const int _sf = cg * 224 + (t) * (64 * 7168); \
        BLDS(rsF0, lds + (par) * K1_BUF + ldsF[0], voffF[0], _sf); \
        BLDS(rsF1, lds + (par) * K1_BUF + ldsF[1], voffF[1], _sf); \
        BLDS(rsF2, lds + (par) * K1_BUF + ldsF[2], voffF[2], _sf); \
        if (wn == 0) BLDS(rsF2, lds + (par) * K1_BUF + ldsF[3], voffF[3], _sf); \
        BLDS(rsS, lds + (par) * K1_BUF + K1_BS + wid * 1024, voffS, sS + (t) * 128); } while (0)

    const u16* pfp; int pfs, pfk, pfb;
    if (bid < 32) { pfp = x; pfs = 11; pfk = 128; pfb = bid * 131072; }
    else if (bid < 144) { pfp = bid < 88 ? w1 : w3; pfs = 7; pfk = 64 * 7168; pfb = (bid < 88 ? bid - 32 : bid - 88) * 8192; }
    else { pfp = bid < 160 ? Bre : Bim; pfs = 11; pfk = 128; pfb = ((bid - 144) & 15) * 131072; }
    const auto rsXB = MKRS(ws + WS_XB, 256 * 32768);
#define PREFETCH(t) do { int _z = threadIdx.x; asm volatile("" : "+v"(_z)); _z &= 63; \
        if (wid == 4) { const auto _r = MKRS(pfp, D_ * F_ * 2); \
            __builtin_amdgcn_raw_ptr_buffer_load_lds(_r, (LAS void*)(lds + K1_PF), 4, _z << pfs, pfb + ((t) + 4) * pfk, 0, 0); \
        } else { const int _u = bid + 256 * (t); const auto _r = MKRS(_u < 896 ? w2 : (_u < 1152 ? Cre : Cim), F_ * D_ * 2); \
            __builtin_amdgcn_raw_ptr_buffer_load_lds(_r, (LAS void*)(lds + K1_PF + 256), 4, _z << 7, (_u < 896 ? _u : (_u - 896) & 255) * 8192, 0, 0); } } while (0)
    const unsigned lb = (unsigned)(size_t)lds;
    const int sw = fq ^ (fr >> 1);
    const int tq = (lane & 15) >> 2, tp = lane & 3;
    const int tbase = K1_W1 + (8 * fq + tq) * 224 + (tp & 1) * (K1_W3 - K1_W1) + 8 * (tp >> 1);
    unsigned aB[2][2], sB[2][2], tB[2][2];
#pragma unroll
    for (int p = 0; p < 2; ++p) {
        aB[p][0] = lb + p * K1_BUF + K1_A + (wm * 64 + fr) * 128 + (sw << 4); aB[p][1] = lb + p * K1_BUF + K1_A + (wm * 64 + fr) * 128 + ((sw ^ 4) << 4);
        sB[p][0] = lb + p * K1_BUF + K1_BS + fr * 128 + (sw << 4); sB[p][1] = lb + p * K1_BUF + K1_BS + fr * 128 + ((sw ^ 4) << 4);
        tB[p][0] = lb + p * K1_BUF + tbase + ((fq & 1) << 4); tB[p][1] = lb + p * K1_BUF + tbase + (((fq & 1) ^ 1) << 4);
    }

    f32x4 acc[4][9];
#pragma unroll
    for (int m = 0; m < 4; ++m)
#pragma unroll
        for (int n = 0; n < 9; ++n) acc[m][n] = (f32x4){0.f, 0.f, 0.f, 0.f};
    bf16x8 Af[4][2], Bs[3][2]; s16x4 Bl[3][2], Bh[3][2];

#define LDA1(par, m) do { DSR128(Af[m][0], aB[par][0], (m) * 2048); DSR128(Af[m][1], aB[par][1], (m) * 2048); } while (0)
#define LDA(par) do { _Pragma("unroll") for (int _m = 0; _m < 4; ++_m) { \
        DSR128(Af[_m][0], aB[par][0], _m * 2048); DSR128(Af[_m][1], aB[par][1], _m * 2048); } } while (0)
#define LDF(par, slot, i) do { \
        DSRTR(Bl[slot][0], tB[par][(i) & 1], ((i) >> 1) * 32); DSRTR(Bh[slot][0], tB[par][(i) & 1], 4 * 224 + ((i) >> 1) * 32); \
        DSRTR(Bl[slot][1], tB[par][(i) & 1], 32 * 224 + ((i) >> 1) * 32); DSRTR(Bh[slot][1], tB[par][(i) & 1], 36 * 224 + ((i) >> 1) * 32); } while (0)
#define LDS_(par, slot, tau) do { DSR128(Bs[slot][0], sB[par][0], (tau) * 2048); DSR128(Bs[slot][1], sB[par][1], (tau) * 2048); } while (0)
#define WA asm volatile("" : "+v"(Af[0][0]), "+v"(Af[0][1]), "+v"(Af[1][0]), "+v"(Af[1][1]), "+v"(Af[2][0]), "+v"(Af[2][1]), "+v"(Af[3][0]), "+v"(Af[3][1]))
#define WF(s) asm volatile("" : "+v"(Bl[s][0]), "+v"(Bh[s][0]), "+v"(Bl[s][1]), "+v"(Bh[s][1]))
#define WS_(s) asm volatile("" : "+v"(Bs[s][0]), "+v"(Bs[s][1]))
#define MMA3(n0, NS) do { __builtin_amdgcn_s_setprio(1); \
        _Pragma("unroll") for (int _k = 0; _k < 2; ++_k) _Pragma("unroll") for (int _m = 0; _m < 4; ++_m) _Pragma("unroll") for (int _n = 0; _n < 3; ++_n) { \
            if ((n0) + _n >= (NS)) acc[_m][(n0) + _n] = __builtin_amdgcn_mfma_f32_16x16x32_bf16(Af[_m][_k], Bs[_n][_k], acc[_m][(n0) + _n], 0, 0, 0); \
            else acc[_m][(n0) + _n] = __builtin_amdgcn_mfma_f32_16x16x32_bf16(CAT(Bl[_n][_k], Bh[_n][_k]), Af[_m][_k], acc[_m][(n0) + _n], 0, 0, 0); } \
        __builtin_amdgcn_s_setprio(0); } while (0)

    { const auto rsW = MKRS(ws, 1u << 20);
      if (wid == 0) BLDS(rsW, lds + K1_G, lane * 16, WS_G + pm * 1024);
      if (wid == 1) BLDS(rsW, lds + K1_CH, (lane & 31) * 16, WS_CH + cg * 512); }
    STAGE_A(0, 0); STAGE_B(0, 0); STAGE_A(1, 1);
    WAIT_V(4); BAR;
    STAMP(1);
    if (wn == 0) {
#define STEP0(par, t, CB, CA) do { \
        LDA(par); LDF(par, 0, 0); LDF(par, 1, 1); LDF(par, 2, 2); \
        BAR; WAIT_L(0); WA; WF(0); WF(1); WF(2); SCHED; MMA3(0, 7); BAR; SCHED; \
        SB(CB, (par) ^ 1, (t) + 1, 0); SB(CB, (par) ^ 1, (t) + 1, 1); LDF(par, 0, 3); SB(CB, (par) ^ 1, (t) + 1, 2); LDF(par, 1, 4); SB(CB, (par) ^ 1, (t) + 1, 3); LDF(par, 2, 5); SB(CB, (par) ^ 1, (t) + 1, 4); \
        BAR; WAIT_L(0); WF(0); WF(1); WF(2); SCHED; MMA3(3, 7); BAR; SCHED; \
        SA(CA, par, (t) + 2, 0); LDF(par, 0, 6); SA(CA, par, (t) + 2, 1); LDS_(par, 1, 0); SA(CA, par, (t) + 2, 2); LDS_(par, 2, 1); SA(CA, par, (t) + 2, 3); \
        BAR; WAIT_L(0); WF(0); WS_(1); WS_(2); SCHED; MMA3(6, 7); if (CA) WAIT_V(4); else WAIT_V(0); BAR; SCHED; } while (0)
        for (int t = 0; t < 14; t += 2) { STEP0(0, t, true, true); STEP0(1, t + 1, true, true); }
        STEP0(0, 14, true, false); STEP0(1, 15, false, false);
    } else {
        BAR;
#define STEP1(par, t, CB, CA) do { \
        SB(CB, (par) ^ 1, (t) + 1, 0); LDA1(par, 0); LDF(par, 0, 7); SB(CB, (par) ^ 1, (t) + 1, 1); LDA1(par, 1); LDF(par, 1, 8); \
        SB(CB, (par) ^ 1, (t) + 1, 2); LDA1(par, 2); LDF(par, 2, 9); SB(CB, (par) ^ 1, (t) + 1, 4); LDA1(par, 3); \
        BAR; WAIT_L(0); WA; WF(0); WF(1); WF(2); SCHED; MMA3(0, 7); BAR; SCHED; \
        SA(CA, par, (t) + 2, 0); LDF(par, 0, 10); SA(CA, par, (t) + 2, 1); LDF(par, 1, 11); SA(CA, par, (t) + 2, 2); LDF(par, 2, 12); SA(CA, par, (t) + 2, 3); \
        BAR; WAIT_L(0); WF(0); WF(1); WF(2); SCHED; MMA3(3, 7); BAR; SCHED; \
        LDF(par, 0, 13); LDS_(par, 1, 2); LDS_(par, 2, 3); \
        if (wid >= 6) { int _z = threadIdx.x; asm volatile("" : "+v"(_z)); unsigned _s = SENT; asm volatile("" : "+v"(_s)); \
            __builtin_amdgcn_raw_buffer_store_b128((u32x4){_s, _s, _s, _s}, rsXB, (_z & 127) * 16, bid * 32768 + (t) * 2048, 0); if (CA) WAIT_V(5); else WAIT_V(0); } \
        else if ((wid == 4 && (t) < 12 && bid < 176) || (wid == 5 && bid + 256 * (t) < 1408)) { PREFETCH(t); WAIT_V(5); } else if (CA) WAIT_V(4); else WAIT_V(0); \
        BAR; WAIT_L(0); WF(0); WS_(1); WS_(2); SCHED; MMA3(6, 7); if (CB) BAR; SCHED; } while (0)
        for (int t = 0; t < 14; t += 2) { STEP1(0, t, true, true); STEP1(1, t + 1, true, true); }
        STEP1(0, 14, true, false); STEP1(1, 15, false, false);
    }

    STAMP(2);
    {
    int tz = threadIdx.x; asm volatile("" : "+v"(tz));
    const int tid = tz, lane = tz & 63, fr = lane & 15, fq = lane >> 4;
    const LAS float* sg = (const LAS float*)(lds + K1_G);
    unsigned char* A2 = ws + WS_A2;
#define FFN_EPI(n, i) do { _Pragma("unroll") for (int _m = 0; _m < 4; ++_m) { \
        const f32x4 _v = acc[_m][n]; \
        const auto _s0 = __builtin_amdgcn_permlane16_swap(__float_as_uint(_v[0]), __float_as_uint(_v[2]), false, false); \
        const auto _s1 = __builtin_amdgcn_permlane16_swap(__float_as_uint(_v[1]), __float_as_uint(_v[3]), false, false); \
        const f32x2 _g = (f32x2){__uint_as_float(_s0[0]), __uint_as_float(_s1[0])}, _u = (f32x2){__uint_as_float(_s0[1]), __uint_as_float(_s1[1])}; \
        const f32x2 _t = _g * (-1.44269504089f); \
        const f32x2 _d = (f32x2){__builtin_amdgcn_exp2f(_t.x), __builtin_amdgcn_exp2f(_t.y)} + 1.0f; \
        const f32x2 _r = (f32x2){__builtin_amdgcn_rcpf(_d.x), __builtin_amdgcn_rcpf(_d.y)}; \
        const f32x2 _h = (_g * _u) * (_r * omg[_m]); \
        *(LAS unsigned*)(lds + (wm * 64 + _m * 16 + fr) * RS + (i) * 16 + fq * 4) = pk_bf16(_h.x, _h.y); } } while (0)

    const LAS f32x4* sch = (const LAS f32x4*)(lds + K1_CH);
    LAS f32x2* sagg = (LAS f32x2*)(lds + K1_AGG);
    f32x2 lam, l16, l64, lq, cf, ex[4], T[4];
    {
        const f32x4 c = sch[wn * 16 + fr];
        lam = (f32x2){c[0], c[1]}; cf = (f32x2){c[2], c[3]};
        const f32x2 l2 = cmul(lam, lam), l4 = cmul(l2, l2), l8 = cmul(l4, l4); l16 = cmul(l8, l8);
        const f32x2 l32 = cmul(l16, l16); l64 = cmul(l32, l32);
        const f32x2 l12 = cmul(l4, l8);
        lq = fq == 0 ? (f32x2){1.f, 0.f} : (fq == 1 ? l4 : (fq == 2 ? l8 : l12));
        f32x2 W = (f32x2){0.f, 0.f};
#pragma unroll
        for (int m = 0; m < 4; ++m) {
            f32x2 a = (f32x2){0.f, 0.f};
#pragma unroll
            for (int j = 0; j < 4; ++j) { const f32x2 u = cmul(cf, (f32x2){acc[m][7][j], acc[m][8][j]}); acc[m][7][j] = u.x; acc[m][8][j] = u.y; a = cfma(lam, a, u); }
            f32x2 x0, x1, x2, x3;
            { const auto p = __builtin_amdgcn_permlane16_swap(__float_as_uint(a.x), __float_as_uint(a.x), false, false);
              const auto q = __builtin_amdgcn_permlane32_swap(p[0], p[0], false, false), r = __builtin_amdgcn_permlane32_swap(p[1], p[1], false, false);
              x0.x = __uint_as_float(q[0]); x2.x = __uint_as_float(q[1]); x1.x = __uint_as_float(r[0]); x3.x = __uint_as_float(r[1]); }
            { const auto p = __builtin_amdgcn_permlane16_swap(__float_as_uint(a.y), __float_as_uint(a.y), false, false);
              const auto q = __builtin_amdgcn_permlane32_swap(p[0], p[0], false, false), r = __builtin_amdgcn_permlane32_swap(p[1], p[1], false, false);
              x0.y = __uint_as_float(q[0]); x2.y = __uint_as_float(q[1]); x1.y = __uint_as_float(r[0]); x3.y = __uint_as_float(r[1]); }
            const f32x2 e2 = cfma(l4, x0, x1), e3 = cfma(l4, e2, x2);
            T[m] = cfma(l4, e3, x3);
            ex[m] = fq == 0 ? (f32x2){0.f, 0.f} : (fq == 1 ? x0 : (fq == 2 ? e2 : e3));
            W = cfma(l16, W, T[m]);
        }
        if (fq == 0) sagg[wm * 32 + wn * 16 + fr] = W;
    }
    STAMP(3); WAIT_L(0); BAR;
    STAMP(4);
    GAS u64* gran = (GAS u64*)(ws + WS_GRAN) + (size_t)cg * 8 * 64;
    f32x2 l256 = (f32x2){0.f, 0.f};
    if (wid == 0) {
        const int ch = lane & 31, part = lane >> 5;
        const f32x4 c = sch[ch];
        f32x2 p = (f32x2){c[0], c[1]};
#pragma unroll
        for (int i = 0; i < 6; ++i) p = cmul(p, p);
        f32x2 a = sagg[ch]; a = cfma(p, a, sagg[32 + ch]); a = cfma(p, a, sagg[64 + ch]); a = cfma(p, a, sagg[96 + ch]);
        l256 = cmul(p, p); l256 = cmul(l256, l256);
        if (pm < 7) __hip_atomic_store(gran + pm * 64 + ch * 2 + part, ((u64)TAG << 32) | __float_as_uint(part ? a.y : a.x), __ATOMIC_RELAXED, __HIP_MEMORY_SCOPE_AGENT);
    }
    {
        float omg[4];
#pragma unroll
        for (int m = 0; m < 4; ++m) omg[m] = 1.f - sg[wm * 64 + m * 16 + fr];
#pragma unroll
        for (int n = 0; n < 7; ++n) FFN_EPI(n, wn * 7 + n);
    }
    const auto rsA2 = MKRS(A2, S_ * 5632 * 2);
    const int rtile = (pm * 2 + (wm >> 1)) * KS2;
    {
        const int j = lane & 7, cf = cg * 14 + wn * 7 + j, r0 = (wm & 1) * 64 + (lane >> 3);
        const unsigned go = (unsigned)(((cf >> 3) * 128 + r0) * 128), cc = cf & 7, rs0 = (lane >> 4);
        const unsigned lo = (unsigned)((wm * 64 + (lane >> 3)) * RS + (wn * 7 + j) * 16);
        WAIT_L(0);
        if (j < 7) {
#pragma unroll
            for (int it = 0; it < 8; ++it) {
                const u32x4 v = *(const LAS u32x4*)(lds + lo + it * 8 * RS);
                __builtin_amdgcn_raw_buffer_store_b128(v, rsA2, go + ((cc ^ ((4 * it + rs0) & 7)) << 4), rtile * 16384 + it * 1024, A2AUX); }
        }
    }
    if (wid == 0) {
        const int ch = lane & 31, part = lane >> 5;
        f32x2 X = (f32x2){0.f, 0.f};
        if (pm > 0) {
            u64 v[7];
            for (unsigned spins = 0;; ++spins) {
                bool ok = true;
#pragma unroll
                for (int j = 0; j < 7; ++j) { const int jj = j < pm ? j : pm - 1;
                    v[j] = __hip_atomic_load(gran + jj * 64 + ch * 2 + part, __ATOMIC_RELAXED, __HIP_MEMORY_SCOPE_AGENT); ok &= (unsigned)(v[j] >> 32) == TAG; }
                if (__all(ok) || spins > (1u << 20)) break;
                __builtin_amdgcn_s_sleep(1);
            }
#pragma unroll
            for (int j = 0; j < 7; ++j) { const float mine = __uint_as_float((unsigned)v[j]), other = __shfl_xor(mine, 32);
                const f32x2 aj = part ? (f32x2){other, mine} : (f32x2){mine, other};
                if (j < pm) X = cfma(l256, X, aj); }
        }
        if (lane < 32) ((LAS f32x2*)(lds + K1_XC))[ch] = X;
    }
    STAMP(5); WAIT_L(0); BAR;
    STAMP(6);
    {
        f32x2 c = ((const LAS f32x2*)(lds + K1_XC))[wn * 16 + fr];
        for (int w = 0; w < wm; ++w) c = cfma(l64, c, sagg[w * 32 + wn * 16 + fr]);
#pragma unroll
        for (int m = 0; m < 4; ++m) {
            f32x2 h = cfma(lq, c, ex[m]);
            const f32x4 g4 = *(const LAS f32x4*)(sg + wm * 64 + m * 16 + fq * 4);
#pragma unroll
            for (int j = 0; j < 4; ++j) {
                h = cfma(lam, h, (f32x2){acc[m][7][j], acc[m][8][j]});
                const float s2 = 2.f * g4[j];
                const unsigned pk = pk_bf16(s2 * h.x, -s2 * h.y);
                LAS unsigned char* dst = lds + (wm * 64 + m * 16 + fq * 4 + j) * RS + 224 + (wn * 16 + fr) * 2;
                *(LAS u16*)dst = (u16)(pk & 0xffffu);
                *(LAS u16*)(dst + 64) = (u16)(pk >> 16);
            }
            c = cfma(l16, c, T[m]);
        }
    }
    STAMP(7); WAIT_L(0);
    STAMP(8);
    {
        const int pc = lane & 3, j = (pc >> 1) * 4 + wn * 2 + (pc & 1), cf = 448 + (pc >> 1) * 128 + cg * 4 + wn * 2 + (pc & 1), r0 = (wm & 1) * 64 + (lane >> 2);
        const unsigned go = (unsigned)(((cf >> 3) * 128 + r0) * 128 + (((cf & 7) ^ ((lane >> 3) & 7)) << 4));
        const unsigned lo = (unsigned)((wm * 64 + (lane >> 2)) * RS + 224 + j * 16);
#pragma unroll
        for (int it = 0; it < 4; ++it) {
            const u32x4 v = *(const LAS u32x4*)(lds + lo + it * 16 * RS);
            __builtin_amdgcn_raw_buffer_store_b128(v, rsA2, go, rtile * 16384 + it * 2048, A2AUX); }
    }
    STAMP(9);
    STAMP(10); STAMP_OUT(0);
    }
}

constexpr int K2_ST = 32768, K2_NST = 5, K2_LDS = K2_NST * K2_ST;

__global__ __launch_bounds__(512, 2) void k2_gemm(const u16* __restrict__ x, const u16* __restrict__ w2, const u16* __restrict__ Cre,
                                                  const u16* __restrict__ Cim, const u16* __restrict__ dsk, unsigned char* __restrict__ ws,
                                                  float* __restrict__ out) {
    extern __shared__ __attribute__((aligned(16))) unsigned char smem[];
    LAS unsigned char* lds = (LAS unsigned char*)smem;
    const int tid = threadIdx.x, wid = __builtin_amdgcn_readfirstlane(tid >> 6), lane = tid & 63;
    const int grp = wid >> 2, wm = (wid >> 1) & 1, wn = wid & 1, fr = lane & 15, fq = lane >> 4;
    const int bid = blockIdx.x, xcd = bid & 7, r_ = bid >> 3, kh = xcd >> 2, sub = xcd & 3;
    const int rt = (sub >> 1) * 8 + (r_ >> 2), ct = (sub & 1) * 4 + (r_ & 3);
    const int s0 = kh * 44;
    constexpr int NS = 44;

    STAMP_DECL; STAMP(0);
    const int w4 = wid & 3;
    const auto rsA = MKRS(ws + WS_A2, S_ * 5632 * 2);
    const int sA = (rt * KS2 + s0) * 16384 + w4 * 4096;
    const unsigned voffL = lane * 16;
    unsigned voffW[2], voffC[2];
#pragma unroll
    for (int i = 0; i < 2; ++i) {
        voffW[i] = (unsigned)((lane >> 4) * 2048 + ((((lane & 15) >> 1) ^ ((lane >> 4) | (i << 2))) << 5) + ((lane & 1) << 4));
        voffC[i] = (unsigned)((lane >> 3) * 2048 + (((lane & 7) ^ ((i * 4 + (lane >> 4)) & 7)) << 4)); }
    const auto rsW = MKRS(w2, F_ * D_ * 2);
    const int sW = w4 * 16 * 2048 + ct * 256 + s0 * (64 * 2048);
    const auto rsCr = MKRS(Cre, D_ * P_ * 2); const auto rsCi = MKRS(Cim, D_ * P_ * 2);
    const int sC = (ct * 128 + w4 * 32) * 2048;
#define BLDS2 BLDS
#define STAGE2(sl, so) do { LAS unsigned char* _d = lds + (so) + w4 * 4096; \
        _Pragma("unroll") for (int _i = 0; _i < 4; ++_i) BLDS2(rsA, _d + _i * 1024, voffL, sA + (sl) * 16384 + _i * 1024); \
        if (s0 + (sl) < 56) { _Pragma("unroll") for (int _i = 0; _i < 4; ++_i) BLDS2(rsW, _d + 16384 + _i * 1024, voffW[_i >> 1], sW + (sl) * (64 * 2048) + _i * 8192); } \
        else { const auto _r = (sl) < 28 ? rsCr : rsCi; \
               _Pragma("unroll") for (int _i = 0; _i < 4; ++_i) BLDS2(_r, _d + 16384 + _i * 1024, voffC[_i & 1], sC + (((sl) - 12) & 15) * 128 + _i * 16384); } } while (0)

    const unsigned lb = (unsigned)(size_t)lds;
    const int sw = (fq ^ (fr >> 1)) << 4;
    const unsigned aoff0 = lb + (wm * 64 + fr) * 128 + sw, aoff1 = lb + (wm * 64 + fr) * 128 + (sw ^ 64);
    const unsigned coff0 = lb + 16384 + (wn * 64 + fr) * 128 + sw, coff1 = lb + 16384 + (wn * 64 + fr) * 128 + (sw ^ 64);
    const int tq = (lane & 15) >> 2, tp = lane & 3, fl = tq | ((fq & 1) << 2);
    unsigned toff[4];
#pragma unroll
    for (int n = 0; n < 4; ++n) toff[n] = lb + 16384 + (8 * fq + tq) * 256 + (((wn * 4 + n) ^ fl) << 5) + 8 * tp;

    f32x4 acc[4][4];
#pragma unroll
    for (int m = 0; m < 4; ++m)
#pragma unroll
        for (int n = 0; n < 4; ++n) acc[m][n] = (f32x4){0.f, 0.f, 0.f, 0.f};
    bf16x8 Af[4][2], Bf[4][2]; s16x4 Bl[4][2], Bh[4][2];

    { LAS unsigned char* d0 = lds + w4 * 4096;
      if (grp == 0) { _Pragma("unroll") for (int i = 0; i < 4; ++i) BLDS2(rsA, d0 + i * 1024, voffL, sA + i * 1024); }
      else { _Pragma("unroll") for (int i = 0; i < 4; ++i) BLDS2(rsW, d0 + 16384 + i * 1024, voffW[i >> 1], sW + i * 8192); } }
    if (grp) STAGE2(2, 2 * K2_ST); else STAGE2(1, K2_ST);
    WAIT_V(8); BAR;
    if (grp) BAR;
    STAMP(1);
    int sl = grp, rdo = grp * K2_ST, sto = (grp + 3) * K2_ST;
#define K2_ADV do { sl += 2; rdo += 2 * K2_ST; if (rdo >= K2_LDS) rdo -= K2_LDS; sto += 2 * K2_ST; if (sto >= K2_LDS) sto -= K2_LDS; } while (0)
#define K2_MMA(BEXPR) do { __builtin_amdgcn_s_setprio(1); \
        _Pragma("unroll") for (int k = 0; k < 2; ++k) _Pragma("unroll") for (int m = 0; m < 4; ++m) _Pragma("unroll") for (int n = 0; n < 4; ++n) \
            acc[m][n] = __builtin_amdgcn_mfma_f32_16x16x32_bf16(BEXPR, Af[m][k], acc[m][n], 0, 0, 0); \
        __builtin_amdgcn_s_setprio(0); } while (0)
#define K2_DMA(TY, i) do { if ((TY) != 2) { LAS unsigned char* _d = lds + sto + w4 * 4096; \
        if ((i) < 4) BLDS2(rsA, _d + (i) * 1024, voffL, sA + (sl + 3) * 16384 + (i) * 1024); \
        else if ((TY) == 0) BLDS2(rsW, _d + 16384 + ((i) - 4) * 1024, voffW[((i) - 4) >> 1], sW + (sl + 3) * (64 * 2048) + ((i) - 4) * 8192); \
        else BLDS2(rsC, _d + 16384 + ((i) - 4) * 1024, voffC[(i) & 1], sC + ((sl + 3 - 12) & 15) * 128 + ((i) - 4) * 16384); } } while (0)
#define K2_STEP(RT, TY) do { \
        const unsigned a0 = aoff0 + rdo, a1 = aoff1 + rdo; const auto rsC = sl + 3 < 28 ? rsCr : rsCi; \
        if ((RT) == 0) { \
            _Pragma("unroll") for (int n = 0; n < 4; ++n) { const unsigned t = toff[n] + rdo; \
                K2_DMA(TY, 2 * n); DSRTR(Bl[n][0], t, 0); DSRTR(Bh[n][0], t, 1024); DSR128(Af[n][0], a0, n * 2048); \
                K2_DMA(TY, 2 * n + 1); DSRTR(Bl[n][1], t, 8192); DSRTR(Bh[n][1], t, 9216); DSR128(Af[n][1], a1, n * 2048); } \
        } else { const unsigned c0 = coff0 + rdo, c1 = coff1 + rdo; \
            _Pragma("unroll") for (int n = 0; n < 4; ++n) { \
                K2_DMA(TY, 2 * n); DSR128(Bf[n][0], c0, n * 2048); DSR128(Af[n][0], a0, n * 2048); \
                K2_DMA(TY, 2 * n + 1); DSR128(Bf[n][1], c1, n * 2048); DSR128(Af[n][1], a1, n * 2048); } } \
        if ((TY) != 2) WAIT_V(8); else WAIT_V(0); \
        BAR; WAIT_L(0); \
        asm volatile("" : "+v"(Af[0][0]), "+v"(Af[0][1]), "+v"(Af[1][0]), "+v"(Af[1][1]), "+v"(Af[2][0]), "+v"(Af[2][1]), "+v"(Af[3][0]), "+v"(Af[3][1])); \
        if ((RT) == 0) { \
            asm volatile("" : "+v"(Bl[0][0]), "+v"(Bh[0][0]), "+v"(Bl[1][0]), "+v"(Bh[1][0]), "+v"(Bl[2][0]), "+v"(Bh[2][0]), "+v"(Bl[3][0]), "+v"(Bh[3][0]), \
                              "+v"(Bl[0][1]), "+v"(Bh[0][1]), "+v"(Bl[1][1]), "+v"(Bh[1][1]), "+v"(Bl[2][1]), "+v"(Bh[2][1]), "+v"(Bl[3][1]), "+v"(Bh[3][1])); \
            SCHED; K2_MMA(CAT(Bl[n][k], Bh[n][k])); \
        } else { \
            asm volatile("" : "+v"(Bf[0][0]), "+v"(Bf[0][1]), "+v"(Bf[1][0]), "+v"(Bf[1][1]), "+v"(Bf[2][0]), "+v"(Bf[2][1]), "+v"(Bf[3][0]), "+v"(Bf[3][1])); \
            SCHED; K2_MMA(Bf[n][k]); } \
        SCHED; K2_ADV; BAR; SCHED; } while (0)
    if (kh == 0) {
        for (; sl < NS - 3;) K2_STEP(0, 0);
        for (; sl < NS;) K2_STEP(0, 2);
    } else {
        for (; sl < 9;) K2_STEP(0, 0);
        for (; sl < 12;) K2_STEP(0, 1);
        for (; sl < NS - 3;) K2_STEP(1, 1);
        for (; sl < NS;) K2_STEP(1, 2);
    }
    STAMP(2);
    if (grp == 0) BAR;
    STAMP(3);
    {
        LAS f32x4* xs = (LAS f32x4*)(lds + ((wid & 3) * 2 + grp) * 8192);
        const LAS f32x4* xr = (const LAS f32x4*)(lds + ((wid & 3) * 2 + (grp ^ 1)) * 8192);
        f32x4 r[2][4];
        if (grp) {
#pragma unroll
            for (int mm = 0; mm < 2; ++mm)
#pragma unroll
                for (int n = 0; n < 4; ++n) { xs[(mm * 4 + n) * 64 + lane] = acc[mm][n]; r[mm][n] = acc[2 + mm][n]; }
        } else {
#pragma unroll
            for (int mm = 0; mm < 2; ++mm)
#pragma unroll
                for (int n = 0; n < 4; ++n) { xs[(mm * 4 + n) * 64 + lane] = acc[2 + mm][n]; r[mm][n] = acc[mm][n]; }
        }
        const int mt = 2 * grp + kh;
        const int row = rt * 128 + wm * 64 + mt * 16 + fr;
        const float g = ((const float*)(ws + WS_G))[row];
        u32x2 xv[4], dv[4];
#pragma unroll
        for (int n = 0; n < 4; ++n) { const int col = ct * 128 + wn * 64 + n * 16 + fq * 4;
            xv[n] = *(const u32x2*)(x + (size_t)row * D_ + col); dv[n] = *(const u32x2*)(dsk + col); }
        WAIT_L(0); BAR;
#pragma unroll
        for (int mm = 0; mm < 2; ++mm)
#pragma unroll
            for (int n = 0; n < 4; ++n) r[mm][n] += xr[(mm * 4 + n) * 64 + lane];
        f32x4 mine[4], give[4];
        if (kh) {
#pragma unroll
            for (int n = 0; n < 4; ++n) { mine[n] = r[1][n]; give[n] = r[0][n]; }
        } else {
#pragma unroll
            for (int n = 0; n < 4; ++n) { mine[n] = r[0][n]; give[n] = r[1][n]; }
        }
        const int pbid = bid ^ 4;
        const auto rs = MKRS(ws + WS_XB, 256 * 32768);
#pragma unroll
        for (int n = 0; n < 4; ++n)
            __builtin_amdgcn_raw_buffer_store_b128(__builtin_bit_cast(u32x4, give[n]), rs, bid * 32768 + wid * 4096 + n * 1024 + lane * 16, 0, 16);
        STAMP(4);
#pragma unroll
        for (int n = 0; n < 4; ++n) {
            mine[n][0] += g * __uint_as_float(xv[n][0] << 16) * __uint_as_float(dv[n][0] << 16);
            mine[n][1] += g * __uint_as_float(xv[n][0] & 0xffff0000u) * __uint_as_float(dv[n][0] & 0xffff0000u);
            mine[n][2] += g * __uint_as_float(xv[n][1] << 16) * __uint_as_float(dv[n][1] << 16);
            mine[n][3] += g * __uint_as_float(xv[n][1] & 0xffff0000u) * __uint_as_float(dv[n][1] & 0xffff0000u); }
        u32x4 pv[4];
        const unsigned char* pp = ws + WS_XB + (size_t)pbid * 32768 + wid * 4096 + lane * 16;
        for (unsigned spins = 0;; ++spins) {
            asm volatile("global_load_dwordx4 %0, %4, off sc1\n\tglobal_load_dwordx4 %1, %4, off offset:1024 sc1\n\t"
                         "global_load_dwordx4 %2, %4, off offset:2048 sc1\n\tglobal_load_dwordx4 %3, %4, off offset:3072 sc1\n\ts_waitcnt vmcnt(0)"
                         : "=&v"(pv[0]), "=&v"(pv[1]), "=&v"(pv[2]), "=&v"(pv[3]) : "v"(pp) : "memory");
            bool ok = true;
#pragma unroll
            for (int n = 0; n < 4; ++n) ok &= (pv[n][0] != SENT) & (pv[n][1] != SENT) & (pv[n][2] != SENT) & (pv[n][3] != SENT);
            if (__all(ok) || spins > (1u << 18)) break;
            __builtin_amdgcn_s_sleep(1);
        }
        STAMP(5);
#pragma unroll
        for (int n = 0; n < 4; ++n)
            *(f32x4*)(out + (size_t)row * D_ + ct * 128 + wn * 64 + n * 16 + fq * 4) = mine[n] + __builtin_bit_cast(f32x4, pv[n]);
        STAMP(6);
        STAMP(7); STAMP_OUT(1);
    }
}
}

extern "C" void kernel_launch(void* const* d_in, const int* in_sizes, int n_in, void* d_out, int out_size, void* d_ws, size_t ws_size,
                              hipStream_t stream) {
    const u16* x = (const u16*)d_in[0]; const u16* lre = (const u16*)d_in[1]; const u16* lim = (const u16*)d_in[2];
    const u16* Bre = (const u16*)d_in[3]; const u16* Bim = (const u16*)d_in[4]; const u16* Cre = (const u16*)d_in[5]; const u16* Cim = (const u16*)d_in[6];
    const u16* ldt = (const u16*)d_in[7]; const u16* dsk = (const u16*)d_in[8];
    const u16* w1 = (const u16*)d_in[10]; const u16* w3 = (const u16*)d_in[11]; const u16* w2 = (const u16*)d_in[12]; const u16* gw = (const u16*)d_in[13];
    unsigned char* ws = (unsigned char*)d_ws;
    (void)hipFuncSetAttribute((const void*)k1_gemm, hipFuncAttributeMaxDynamicSharedMemorySize, K1_LDS);
    (void)hipFuncSetAttribute((const void*)k2_gemm, hipFuncAttributeMaxDynamicSharedMemorySize, K2_LDS);
    k0_prep<<<256, 512, 0, stream>>>(x, gw, lre, lim, ldt, w1, w3, Bre, Bim, ws);
    k1_gemm<<<256, 512, K1_LDS, stream>>>(x, w1, w3, Bre, Bim, w2, Cre, Cim, ws);
    k2_gemm<<<256, 512, K2_LDS, stream>>>(x, w2, Cre, Cim, dsk, ws, (float*)d_out);
}
```

```cpp
#include <hip/hip_runtime.h>
#include <hip/hip_bf16.h>
#include <stdint.h>

#define LAS __attribute__((address_space(3)))
#define GAS __attribute__((address_space(1)))
typedef unsigned short u16;
typedef unsigned long long u64;
typedef short bf16x8 __attribute__((ext_vector_type(8)));
typedef short s16x4 __attribute__((ext_vector_type(4)));
typedef float f32x4 __attribute__((ext_vector_type(4)));
typedef float f32x2 __attribute__((ext_vector_type(2)));
typedef unsigned u32x4 __attribute__((ext_vector_type(4)));
typedef unsigned u32x2 __attribute__((ext_vector_type(2)));

#define A2AUX 0
namespace {
constexpr int S_ = 2048, D_ = 1024, P_ = 1024, F_ = 3584;
constexpr int KS2 = 88;
constexpr size_t WS_GRAN = 0;
constexpr size_t WS_FLAG = 131072;
constexpr size_t WS_ZERO_BYTES = 131072 + 16384;
constexpr size_t WS_G = 262144;
constexpr size_t WS_CH = 262144 + 8192;
constexpr size_t WS_A2 = 1u << 20;
constexpr size_t WS_XB = 32u << 20;
constexpr unsigned TAG = 0x5A5A0001u;
constexpr unsigned SENT = 0xFFFFFFFFu;

__device__ __forceinline__ float bf2f(u16 v) { return __uint_as_float(((unsigned)v) << 16); }
__device__ __forceinline__ unsigned pk_bf16(float lo, float hi) {
    unsigned r; asm("v_cvt_pk_bf16_f32 %0, %1, %2" : "=v"(r) : "v"(lo), "v"(hi)); return r; }
__device__ __forceinline__ f32x2 cfma(f32x2 a, f32x2 b, f32x2 c) {
    const f32x2 t = __builtin_elementwise_fma((f32x2){a.x, a.x}, b, c);
    return __builtin_elementwise_fma((f32x2){-a.y, a.y}, (f32x2){b.y, b.x}, t); }
__device__ __forceinline__ f32x2 cmul(f32x2 a, f32x2 b) {
    const f32x2 t = (f32x2){a.x, a.x} * b;
    return __builtin_elementwise_fma((f32x2){-a.y, a.y}, (f32x2){b.y, b.x}, t); }

#define STAMP(i)
#define STAMP_DECL
#define STAMP_OUT(kern)
#define WAIT_V(n) asm volatile("s_waitcnt vmcnt(" #n ")" ::: "memory")
#define WAIT_L(n) asm volatile("s_waitcnt lgkmcnt(" #n ")" ::: "memory")
#define BAR __builtin_amdgcn_s_barrier()
#define SCHED __builtin_amdgcn_sched_barrier(0)
#define DSR128(dst, addr, off) asm volatile("ds_read_b128 %0, %1 offset:%2" : "=v"(dst) : "v"(addr), "i"(off))
#define DSRTR(dst, addr, off) asm volatile("ds_read_b64_tr_b16 %0, %1 offset:%2" : "=v"(dst) : "v"(addr), "i"(off))
#define CAT(lo, hi) __builtin_shufflevector(lo, hi, 0, 1, 2, 3, 4, 5, 6, 7)
#define MKRS(p, bytes) __builtin_amdgcn_make_buffer_rsrc((void*)(p), 0, (int)(bytes), 0x00020000)
#define BLDS(rs, dst, voff, soff) __builtin_amdgcn_raw_ptr_buffer_load_lds((rs), (LAS void*)(dst), 16, (int)(voff), (int)(soff), 0, 0)

__global__ __launch_bounds__(512) void k0_prep(const u16* __restrict__ x, const u16* __restrict__ gw, const u16* __restrict__ lre,
                                               const u16* __restrict__ lim, const u16* __restrict__ ldt, const u16* __restrict__ w1,
                                               const u16* __restrict__ w3, const u16* __restrict__ Bre, const u16* __restrict__ Bim,
                                               unsigned char* __restrict__ ws) {
    const int tid = threadIdx.x, wid = tid >> 6, lane = tid & 63, bid = blockIdx.x;
    unsigned pfv = 0;
    if (tid < 144) {
        const int L = bid * 144 + tid;
        const char* p;
        if (L < 28672) { const int j = L < 14336 ? L : L - 14336; p = (const char*)(L < 14336 ? w1 : w3) + (size_t)j * 128; }
        else { const int j = L - 28672, r = (j >> 2) & 1023; p = (const char*)(j < 4096 ? Bre : Bim) + (size_t)r * 2048 + (j & 3) * 128; }
        pfv = *(const unsigned*)p;
    }
    const int row = bid * 8 + wid;
    const u32x4* xr = (const u32x4*)(x + (size_t)row * D_);
    const u32x4* gr = (const u32x4*)gw;
    float s = 0.f;
#pragma unroll
    for (int i = 0; i < 2; ++i) {
        const u32x4 a = xr[lane + 64 * i], b = gr[lane + 64 * i];
#pragma unroll
        for (int j = 0; j < 4; ++j) {
            s += __uint_as_float(a[j] << 16) * __uint_as_float(b[j] << 16);
            s += __uint_as_float(a[j] & 0xffff0000u) * __uint_as_float(b[j] & 0xffff0000u);
        }
    }
#pragma unroll
    for (int o = 32; o >= 1; o >>= 1) s += __shfl_xor(s, o);
    if (lane == 0) ((float*)(ws + WS_G))[row] = 1.f / (1.f + __expf(-s));
    if (tid < 36) ((u32x4*)(ws + (size_t)bid * 576))[tid] = (u32x4){0u, 0u, 0u, 0u};
    asm volatile("" :: "v"(pfv));
    if (bid < 2) {
        const int p = bid * 512 + tid;
        const float lr = bf2f(lre[p]), li = bf2f(lim[p]), dt = expf(bf2f(ldt[p]));
        const float er = expf(dt * lr); float sn, cs; sincosf(dt * li, &sn, &cs);
        const float br = er * cs, bi = er * sn;
        const float nr = br - 1.f, ni = bi, den = lr * lr + li * li;
        ((f32x4*)(ws + WS_CH))[p] = (f32x4){br, bi, (nr * lr + ni * li) / den, (ni * lr - nr * li) / den};
    }
}

constexpr int K1_A = 0, K1_W1 = 32768, K1_W3 = 32768 + 14336 + 128, K1_BS = K1_W3 + 14336, K1_BUF = K1_BS + 8192;
constexpr int K1_X0 = 2 * K1_BUF;
constexpr int K1_G = K1_X0, K1_CH = K1_X0 + 1024, K1_AGG = K1_X0 + 2048, K1_XC = K1_X0 + 3072, K1_PF = K1_X0 + 3328, K1_LDS = K1_X0 + 3840;
constexpr int RS = 368;

__device__ __forceinline__ size_t a2_addr(int R, int cf) {
    const int rt = R >> 7, rr = R & 127, s = cf >> 3, c = cf & 7;
    return ((size_t)(rt * KS2 + s) * 128 + rr) * 128 + ((c ^ ((rr >> 1) & 7)) << 4);
}

__global__ __launch_bounds__(512, 2) void k1_gemm(const u16* __restrict__ x, const u16* __restrict__ w1, const u16* __restrict__ w3,
                                                  const u16* __restrict__ Bre, const u16* __restrict__ Bim, const u16* __restrict__ w2,
                                                  const u16* __restrict__ Cre, const u16* __restrict__ Cim, unsigned char* __restrict__ ws) {
    extern __shared__ __attribute__((aligned(16))) unsigned char smem[];
    LAS unsigned char* lds = (LAS unsigned char*)smem;
    const int tid = threadIdx.x, wid = __builtin_amdgcn_readfirstlane(tid >> 6), lane = tid & 63;
    const int wm = wid & 3, wn = wid >> 2, fr = lane & 15, fq = lane >> 4;
    const int bid = blockIdx.x, xcd = bid & 7, rr_ = bid >> 3, cg = xcd * 4 + (rr_ & 3), pm = rr_ >> 2;

    STAMP_DECL; STAMP(0);
    const auto rsX = MKRS(x, S_ * D_ * 2);
    const int sA = pm * 524288 + wid * 65536;
    unsigned voffA[2];
#pragma unroll
    for (int i = 0; i < 2; ++i) voffA[i] = (unsigned)((lane >> 3) * 2048 + (((lane & 7) ^ ((i * 4 + (lane >> 4)) & 7)) << 4));
    unsigned voffF[4]; int ldsF[4];
    const auto rsF0 = MKRS(w1, D_ * F_ * 2);
    const auto rsF1 = MKRS(wid + 8 < 14 ? w1 : w3, D_ * F_ * 2);
    const auto rsF2 = MKRS(w3, D_ * F_ * 2);
#pragma unroll
    for (int jj = 0; jj < 4; ++jj) {
        int j = wid + 8 * jj; if (j >= 28) j = 16 + wid;
        const int mat = j / 14, v = j % 14, idx = 64 * v + lane, k = idx / 14, pc = idx % 14;
        voffF[jj] = (unsigned)(k * 7168 + ((pc ^ ((k >> 3) & 1)) << 4));
        ldsF[jj] = (mat ? K1_W3 : K1_W1) + v * 1024;
    }
    const unsigned voffS = (unsigned)((8 * (wid & 1) + (lane >> 3)) * 2048 + (((lane & 7) ^ ((4 * (wid & 1) + (lane >> 4)) & 7)) << 4));
    const auto rsS = MKRS(((wid >> 1) & 1) ? Bim : Bre, P_ * D_ * 2);
    const int sS = (cg * 32 + 16 * (wid >> 2)) * 2048;

#define SA(c, par, t, i) do { if (c) BLDS(rsX, lds + (par) * K1_BUF + K1_A + (wid * 4 + (i)) * 1024, voffA[(i) & 1], sA + (t) * 128 + (i) * 16384); } while (0)
#define SB(c, par, t, j) do { if (c) { const int _sf = cg * 224 + (t) * (64 * 7168); \
        if ((j) == 0) BLDS(rsF0, lds + (par) * K1_BUF + ldsF[0], voffF[0], _sf); \
        if ((j) == 1) BLDS(rsF1, lds + (par) * K1_BUF + ldsF[1], voffF[1], _sf); \
        if ((j) == 2) BLDS(rsF2, lds + (par) * K1_BUF + ldsF[2], voffF[2], _sf); \
        if ((j) == 3) BLDS(rsF2, lds + (par) * K1_BUF + ldsF[3], voffF[3], _sf); \
        if ((j) == 4) BLDS(rsS, lds + (par) * K1_BUF + K1_BS + wid * 1024, voffS, sS + (t) * 128); } } while (0)
#define STAGE_A(par, t) do { _Pragma("unroll") for (int _i = 0; _i < 4; ++_i) \
        BLDS(rsX, lds + (par) * K1_BUF + K1_A + (wid * 4 + _i) * 1024, voffA[_i & 1], sA + (t) * 128 + _i * 16384); } while (0)
#define STAGE_B(par, t) do { const int _sf = cg * 224 + (t) * (64 * 7168); \
        BLDS(rsF0, lds + (par) * K1_BUF + ldsF[0], voffF[0], _sf); \
        BLDS(rsF1, lds + (par) * K1_BUF + ldsF[1], voffF[1], _sf); \
        BLDS(rsF2, lds + (par) * K1_BUF + ldsF[2], voffF[2], _sf); \
        if (wn == 0) BLDS(rsF2, lds + (par) * K1_BUF + ldsF[3], voffF[3], _sf); \
        BLDS(rsS, lds + (par) * K1_BUF + K1_BS + wid * 1024, voffS, sS + (t) * 128); } while (0)

    const u16* pfp; int pfs, pfk, pfb;
    if (bid < 32) { pfp = x; pfs = 11; pfk = 128; pfb = bid * 131072; }
    else if (bid < 144) { pfp = bid < 88 ? w1 : w3; pfs = 7; pfk = 64 * 7168; pfb = (bid < 88 ? bid - 32 : bid - 88) * 8192; }
    else { pfp = bid < 160 ? Bre : Bim; pfs = 11; pfk = 128; pfb = ((bid - 144) & 15) * 131072; }
    const auto rsXB = MKRS(ws + WS_XB, 256 * 32768);
#define PREFETCH(t) do { int _z = threadIdx.x; asm volatile("" : "+v"(_z)); _z &= 63; \
        if (wid == 4) { const auto _r = MKRS(pfp, D_ * F_ * 2); \
            __builtin_amdgcn_raw_ptr_buffer_load_lds(_r, (LAS void*)(lds + K1_PF), 4, _z << pfs, pfb + ((t) + 4) * pfk, 0, 0); \
        } else { const int _u = bid + 256 * (t); const auto _r = MKRS(_u < 896 ? w2 : (_u < 1152 ? Cre : Cim), F_ * D_ * 2); \
            __builtin_amdgcn_raw_ptr_buffer_load_lds(_r, (LAS void*)(lds + K1_PF + 256), 4, _z << 7, (_u < 896 ? _u : (_u - 896) & 255) * 8192, 0, 0); } } while (0)
    const unsigned lb = (unsigned)(size_t)lds;
    const int sw = fq ^ (fr >> 1);
    const int tq = (lane & 15) >> 2, tp = lane & 3;
    const int tbase = K1_W1 + (8 * fq + tq) * 224 + (tp & 1) * (K1_W3 - K1_W1) + 8 * (tp >> 1);
    unsigned aB[2][2], sB[2][2], tB[2][2];
#pragma unroll
    for (int p = 0; p < 2; ++p) {
        aB[p][0] = lb + p * K1_BUF + K1_A + (wm * 64 + fr) * 128 + (sw << 4); aB[p][1] = lb + p * K1_BUF + K1_A + (wm * 64 + fr) * 128 + ((sw ^ 4) << 4);
        sB[p][0] = lb + p * K1_BUF + K1_BS + fr * 128 + (sw << 4); sB[p][1] = lb + p * K1_BUF + K1_BS + fr * 128 + ((sw ^ 4) << 4);
        tB[p][0] = lb + p * K1_BUF + tbase + ((fq & 1) << 4); tB[p][1] = lb + p * K1_BUF + tbase + (((fq & 1) ^ 1) << 4);
    }

    f32x4 acc[4][9];
#pragma unroll
    for (int m = 0; m < 4; ++m)
#pragma unroll
        for (int n = 0; n < 9; ++n) acc[m][n] = (f32x4){0.f, 0.f, 0.f, 0.f};
    bf16x8 Af[4][2], Bs[3][2]; s16x4 Bl[3][2], Bh[3][2];

#define LDA1(par, m) do { DSR128(Af[m][0], aB[par][0], (m) * 2048); DSR128(Af[m][1], aB[par][1], (m) * 2048); } while (0)
#define LDA(par) do { _Pragma("unroll") for (int _m = 0; _m < 4; ++_m) { \
        DSR128(Af[_m][0], aB[par][0], _m * 2048); DSR128(Af[_m][1], aB[par][1], _m * 2048); } } while (0)
#define LDF(par, slot, i) do { \
        DSRTR(Bl[slot][0], tB[par][(i) & 1], ((i) >> 1) * 32); DSRTR(Bh[slot][0], tB[par][(i) & 1], 4 * 224 + ((i) >> 1) * 32); \
        DSRTR(Bl[slot][1], tB[par][(i) & 1], 32 * 224 + ((i) >> 1) * 32); DSRTR(Bh[slot][1], tB[par][(i) & 1], 36 * 224 + ((i) >> 1) * 32); } while (0)
#define LDS_(par, slot, tau) do { DSR128(Bs[slot][0], sB[par][0], (tau) * 2048); DSR128(Bs[slot][1], sB[par][1], (tau) * 2048); } while (0)
#define WA asm volatile("" : "+v"(Af[0][0]), "+v"(Af[0][1]), "+v"(Af[1][0]), "+v"(Af[1][1]), "+v"(Af[2][0]), "+v"(Af[2][1]), "+v"(Af[3][0]), "+v"(Af[3][1]))
#define WF(s) asm volatile("" : "+v"(Bl[s][0]), "+v"(Bh[s][0]), "+v"(Bl[s][1]), "+v"(Bh[s][1]))
#define WS_(s) asm volatile("" : "+v"(Bs[s][0]), "+v"(Bs[s][1]))
#define MMA3(n0, NS) do { __builtin_amdgcn_s_setprio(1); \
        _Pragma("unroll") for (int _k = 0; _k < 2; ++_k) _Pragma("unroll") for (int _m = 0; _m < 4; ++_m) _Pragma("unroll") for (int _n = 0; _n < 3; ++_n) { \
            if ((n0) + _n >= (NS)) acc[_m][(n0) + _n] = __builtin_amdgcn_mfma_f32_16x16x32_bf16(Af[_m][_k], Bs[_n][_k], acc[_m][(n0) + _n], 0, 0, 0); \
            else acc[_m][(n0) + _n] = __builtin_amdgcn_mfma_f32_16x16x32_bf16(CAT(Bl[_n][_k], Bh[_n][_k]), Af[_m][_k], acc[_m][(n0) + _n], 0, 0, 0); } \
        __builtin_amdgcn_s_setprio(0); } while (0)

    { const auto rsW = MKRS(ws, 1u << 20);
      if (wid == 0) BLDS(rsW, lds + K1_G, lane * 16, WS_G + pm * 1024);
      if (wid == 1) BLDS(rsW, lds + K1_CH, (lane & 31) * 16, WS_CH + cg * 512); }
    STAGE_A(0, 0); STAGE_B(0, 0); STAGE_A(1, 1);
    WAIT_V(4); BAR;
    STAMP(1);
    if (wn == 0) {
#define STEP0(par, t, CB, CA) do { \
        LDA(par); LDF(par, 0, 0); LDF(par, 1, 1); LDF(par, 2, 2); \
        BAR; WAIT_L(0); WA; WF(0); WF(1); WF(2); SCHED; MMA3(0, 7); BAR; SCHED; \
        SB(CB, (par) ^ 1, (t) + 1, 0); SB(CB, (par) ^ 1, (t) + 1, 1); LDF(par, 0, 3); SB(CB, (par) ^ 1, (t) + 1, 2); LDF(par, 1, 4); SB(CB, (par) ^ 1, (t) + 1, 3); LDF(par, 2, 5); SB(CB, (par) ^ 1, (t) + 1, 4); \
        BAR; WAIT_L(0); WF(0); WF(1); WF(2); SCHED; MMA3(3, 7); BAR; SCHED; \
        SA(CA, par, (t) + 2, 0); LDF(par, 0, 6); SA(CA, par, (t) + 2, 1); LDS_(par, 1, 0); SA(CA, par, (t) + 2, 2); LDS_(par, 2, 1); SA(CA, par, (t) + 2, 3); \
        BAR; WAIT_L(0); WF(0); WS_(1); WS_(2); SCHED; MMA3(6, 7); if (CA) WAIT_V(4); else WAIT_V(0); BAR; SCHED; } while (0)
        for (int t = 0; t < 14; t += 2) { STEP0(0, t, true, true); STEP0(1, t + 1, true, true); }
        STEP0(0, 14, true, false); STEP0(1, 15, false, false);
    } else {
        BAR;
#define STEP1(par, t, CB, CA) do { \
        SB(CB, (par) ^ 1, (t) + 1, 0); LDA1(par, 0); LDF(par, 0, 7); SB(CB, (par) ^ 1, (t) + 1, 1); LDA1(par, 1); LDF(par, 1, 8); \
        SB(CB, (par) ^ 1, (t) + 1, 2); LDA1(par, 2); LDF(par, 2, 9); SB(CB, (par) ^ 1, (t) + 1, 4); LDA1(par, 3); \
        BAR; WAIT_L(0); WA; WF(0); WF(1); WF(2); SCHED; MMA3(0, 7); BAR; SCHED; \
        SA(CA, par, (t) + 2, 0); LDF(par, 0, 10); SA(CA, par, (t) + 2, 1); LDF(par, 1, 11); SA(CA, par, (t) + 2, 2); LDF(par, 2, 12); SA(CA, par, (t) + 2, 3); \
        BAR; WAIT_L(0); WF(0); WF(1); WF(2); SCHED; MMA3(3, 7); BAR; SCHED; \
        LDF(par, 0, 13); LDS_(par, 1, 2); LDS_(par, 2, 3); \
        if (wid >= 6) { int _z = threadIdx.x; asm volatile("" : "+v"(_z)); unsigned _s = SENT; asm volatile("" : "+v"(_s)); \
            __builtin_amdgcn_raw_buffer_store_b128((u32x4){_s, _s, _s, _s}, rsXB, (_z & 127) * 16, bid * 32768 + (t) * 2048, 0); if (CA) WAIT_V(5); else WAIT_V(0); } \
        else if ((wid == 4 && (t) < 12 && bid < 176) || (wid == 5 && bid + 256 * (t) < 1408)) { PREFETCH(t); WAIT_V(5); } else if (CA) WAIT_V(4); else WAIT_V(0); \
        BAR; WAIT_L(0); WF(0); WS_(1); WS_(2); SCHED; MMA3(6, 7); if (CB) BAR; SCHED; } while (0)
        for (int t = 0; t < 14; t += 2) { STEP1(0, t, true, true); STEP1(1, t + 1, true, true); }
        STEP1(0, 14, true, false); STEP1(1, 15, false, false);
    }

    STAMP(2);
    {
    int tz = threadIdx.x; asm volatile("" : "+v"(tz));
    const int tid = tz, lane = tz & 63, fr = lane & 15, fq = lane >> 4;
    const LAS float* sg = (const LAS float*)(lds + K1_G);
    unsigned char* A2 = ws + WS_A2;
#define FFN_EPI(n, i) do { _Pragma("unroll") for (int _m = 0; _m < 4; ++_m) { \
        const f32x4 _v = acc[_m][n]; \
        const auto _s0 = __builtin_amdgcn_permlane16_swap(__float_as_uint(_v[0]), __float_as_uint(_v[2]), false, false); \
        const auto _s1 = __builtin_amdgcn_permlane16_swap(__float_as_uint(_v[1]), __float_as_uint(_v[3]), false, false); \
        const f32x2 _g = (f32x2){__uint_as_float(_s0[0]), __uint_as_float(_s1[0])}, _u = (f32x2){__uint_as_float(_s0[1]), __uint_as_float(_s1[1])}; \
        const f32x2 _t = _g * (-1.44269504089f); \
        const f32x2 _d = (f32x2){__builtin_amdgcn_exp2f(_t.x), __builtin_amdgcn_exp2f(_t.y)} + 1.0f; \
        const f32x2 _r = (f32x2){__builtin_amdgcn_rcpf(_d.x), __builtin_amdgcn_rcpf(_d.y)}; \
        const f32x2 _h = (_g * _u) * (_r * omg[_m]); \
        *(LAS unsigned*)(lds + (wm * 64 + _m * 16 + fr) * RS + (i) * 16 + fq * 4) = pk_bf16(_h.x, _h.y); } } while (0)

    const LAS f32x4* sch = (const LAS f32x4*)(lds + K1_CH);
    LAS f32x2* sagg = (LAS f32x2*)(lds + K1_AGG);
    f32x2 lam, l16, l64, lq, cf, ex[4], T[4];
    {
        const f32x4 c = sch[wn * 16 + fr];
        lam = (f32x2){c[0], c[1]}; cf = (f32x2){c[2], c[3]};
        const f32x2 l2 = cmul(lam, lam), l4 = cmul(l2, l2), l8 = cmul(l4, l4); l16 = cmul(l8, l8);
        const f32x2 l32 = cmul(l16, l16); l64 = cmul(l32, l32);
        const f32x2 l12 = cmul(l4, l8);
        lq = fq == 0 ? (f32x2){1.f, 0.f} : (fq == 1 ? l4 : (fq == 2 ? l8 : l12));
        f32x2 W = (f32x2){0.f, 0.f};
#pragma unroll
        for (int m = 0; m < 4; ++m) {
            f32x2 a = (f32x2){0.f, 0.f};
#pragma unroll
            for (int j = 0; j < 4; ++j) { const f32x2 u = cmul(cf, (f32x2){acc[m][7][j], acc[m][8][j]}); acc[m][7][j] = u.x; acc[m][8][j] = u.y; a = cfma(lam, a, u); }
            f32x2 x0, x1, x2, x3;
            { const auto p = __builtin_amdgcn_permlane16_swap(__float_as_uint(a.x), __float_as_uint(a.x), false, false);
              const auto q = __builtin_amdgcn_permlane32_swap(p[0], p[0], false, false), r = __builtin_amdgcn_permlane32_swap(p[1], p[1], false, false);
              x0.x = __uint_as_float(q[0]); x2.x = __uint_as_float(q[1]); x1.x = __uint_as_float(r[0]); x3.x = __uint_as_float(r[1]); }
            { const auto p = __builtin_amdgcn_permlane16_swap(__float_as_uint(a.y), __float_as_uint(a.y), false, false);
              const auto q = __builtin_amdgcn_permlane32_swap(p[0], p[0], false, false), r = __builtin_amdgcn_permlane32_swap(p[1], p[1], false, false);
              x0.y = __uint_as_float(q[0]); x2.y = __uint_as_float(q[1]); x1.y = __uint_as_float(r[0]); x3.y = __uint_as_float(r[1]); }
            const f32x2 e2 = cfma(l4, x0, x1), e3 = cfma(l4, e2, x2);
            T[m] = cfma(l4, e3, x3);
            ex[m] = fq == 0 ? (f32x2){0.f, 0.f} : (fq == 1 ? x0 : (fq == 2 ? e2 : e3));
            W = cfma(l16, W, T[m]);
        }
        if (fq == 0) sagg[wm * 32 + wn * 16 + fr] = W;
    }
    STAMP(3); WAIT_L(0); BAR;
    STAMP(4);
    GAS u64* gran = (GAS u64*)(ws + WS_GRAN) + (size_t)cg * 8 * 64;
    f32x2 l256 = (f32x2){0.f, 0.f};
    if (wid == 0) {
        const int ch = lane & 31, part = lane >> 5;
        const f32x4 c = sch[ch];
        f32x2 p = (f32x2){c[0], c[1]};
#pragma unroll
        for (int i = 0; i < 6; ++i) p = cmul(p, p);
        f32x2 a = sagg[ch]; a = cfma(p, a, sagg[32 + ch]); a = cfma(p, a, sagg[64 + ch]); a = cfma(p, a, sagg[96 + ch]);
        l256 = cmul(p, p); l256 = cmul(l256, l256);
        if (pm < 7) __hip_atomic_store(gran + pm * 64 + ch * 2 + part, ((u64)TAG << 32) | __float_as_uint(part ? a.y : a.x), __ATOMIC_RELAXED, __HIP_MEMORY_SCOPE_AGENT);
    }
    {
        float omg[4];
#pragma unroll
        for (int m = 0; m < 4; ++m) omg[m] = 1.f - sg[wm * 64 + m * 16 + fr];
#pragma unroll
        for (int n = 0; n < 7; ++n) FFN_EPI(n, wn * 7 + n);
    }
    const auto rsA2 = MKRS(A2, S_ * 5632 * 2);
    const int rtile = (pm * 2 + (wm >> 1)) * KS2;
    {
        const int j = lane & 7, cf = cg * 14 + wn * 7 + j, r0 = (wm & 1) * 64 + (lane >> 3);
        const unsigned go = (unsigned)(((cf >> 3) * 128 + r0) * 128), cc = cf & 7, rs0 = (lane >> 4);
        const unsigned lo = (unsigned)((wm * 64 + (lane >> 3)) * RS + (wn * 7 + j) * 16);
        WAIT_L(0);
        if (j < 7) {
#pragma unroll
            for (int it = 0; it < 8; ++it) {
                const u32x4 v = *(const LAS u32x4*)(lds + lo + it * 8 * RS);
                __builtin_amdgcn_raw_buffer_store_b128(v, rsA2, go + ((cc ^ ((4 * it + rs0) & 7)) << 4), rtile * 16384 + it * 1024, A2AUX); }
        }
    }
    if (wid == 0) {
        const int ch = lane & 31, part = lane >> 5;
        f32x2 X = (f32x2){0.f, 0.f};
        if (pm > 0) {
            u64 v[7];
            for (unsigned spins = 0;; ++spins) {
                bool ok = true;
#pragma unroll
                for (int j = 0; j < 7; ++j) { const int jj = j < pm ? j : pm - 1;
                    v[j] = __hip_atomic_load(gran + jj * 64 + ch * 2 + part, __ATOMIC_RELAXED, __HIP_MEMORY_SCOPE_AGENT); ok &= (unsigned)(v[j] >> 32) == TAG; }
                if (__all(ok) || spins > (1u << 20)) break;
                __builtin_amdgcn_s_sleep(1);
            }
#pragma unroll
            for (int j = 0; j < 7; ++j) { const float mine = __uint_as_float((unsigned)v[j]), other = __shfl_xor(mine, 32);
                const f32x2 aj = part ? (f32x2){other, mine} : (f32x2){mine, other};
                if (j < pm) X = cfma(l256, X, aj); }
        }
        if (lane < 32) ((LAS f32x2*)(lds + K1_XC))[ch] = X;
    }
    STAMP(5); WAIT_L(0); BAR;
    STAMP(6);
    {
        f32x2 c = ((const LAS f32x2*)(lds + K1_XC))[wn * 16 + fr];
        for (int w = 0; w < wm; ++w) c = cfma(l64, c, sagg[w * 32 + wn * 16 + fr]);
#pragma unroll
        for (int m = 0; m < 4; ++m) {
            f32x2 h = cfma(lq, c, ex[m]);
            const f32x4 g4 = *(const LAS f32x4*)(sg + wm * 64 + m * 16 + fq * 4);
#pragma unroll
            for (int j = 0; j < 4; ++j) {
                h = cfma(lam, h, (f32x2){acc[m][7][j], acc[m][8][j]});
                const f32x2 hs = h * ((f32x2){2.f, -2.f} * g4[j]);
                const unsigned pk = pk_bf16(hs.x, hs.y);
                LAS unsigned char* dst = lds + (wm * 64 + m * 16 + fq * 4 + j) * RS + 224 + (wn * 16 + fr) * 2;
                *(LAS u16*)dst = (u16)(pk & 0xffffu);
                *(LAS u16*)(dst + 64) = (u16)(pk >> 16);
            }
            c = cfma(l16, c, T[m]);
        }
    }
    STAMP(7); WAIT_L(0);
    STAMP(8);
    {
        const int pc = lane & 3, j = (pc >> 1) * 4 + wn * 2 + (pc & 1), cf = 448 + (pc >> 1) * 128 + cg * 4 + wn * 2 + (pc & 1), r0 = (wm & 1) * 64 + (lane >> 2);
        const unsigned go = (unsigned)(((cf >> 3) * 128 + r0) * 128 + (((cf & 7) ^ ((lane >> 3) & 7)) << 4));
        const unsigned lo = (unsigned)((wm * 64 + (lane >> 2)) * RS + 224 + j * 16);
#pragma unroll
        for (int it = 0; it < 4; ++it) {
            const u32x4 v = *(const LAS u32x4*)(lds + lo + it * 16 * RS);
            __builtin_amdgcn_raw_buffer_store_b128(v, rsA2, go, rtile * 16384 + it * 2048, A2AUX); }
    }
    STAMP(9);
    STAMP(10); STAMP_OUT(0);
    }
}

constexpr int K2_ST = 32768, K2_NST = 5, K2_LDS = K2_NST * K2_ST;

__global__ __launch_bounds__(512, 2) void k2_gemm(const u16* __restrict__ x, const u16* __restrict__ w2, const u16* __restrict__ Cre,
                                                  const u16* __restrict__ Cim, const u16* __restrict__ dsk, unsigned char* __restrict__ ws,
                                                  float* __restrict__ out) {
    extern __shared__ __attribute__((aligned(16))) unsigned char smem[];
    LAS unsigned char* lds = (LAS unsigned char*)smem;
    const int tid = threadIdx.x, wid = __builtin_amdgcn_readfirstlane(tid >> 6), lane = tid & 63;
    const int grp = wid >> 2, wm = (wid >> 1) & 1, wn = wid & 1, fr = lane & 15, fq = lane >> 4;
    const int bid = blockIdx.x, xcd = bid & 7, r_ = bid >> 3, kh = xcd >> 2, sub = xcd & 3;
    const int rt = (sub >> 1) * 8 + (r_ >> 2), ct = (sub & 1) * 4 + (r_ & 3);
    const int s0 = kh * 44;
    constexpr int NS = 44;

    STAMP_DECL; STAMP(0);
    const int w4 = wid & 3;
    const auto rsA = MKRS(ws + WS_A2, S_ * 5632 * 2);
    const int sA = (rt * KS2 + s0) * 16384 + w4 * 4096;
    const unsigned voffL = lane * 16;
    unsigned voffW[2], voffC[2];
#pragma unroll
    for (int i = 0; i < 2; ++i) {
        voffW[i] = (unsigned)((lane >> 4) * 2048 + ((((lane & 15) >> 1) ^ ((lane >> 4) | (i << 2))) << 5) + ((lane & 1) << 4));
        voffC[i] = (unsigned)((lane >> 3) * 2048 + (((lane & 7) ^ ((i * 4 + (lane >> 4)) & 7)) << 4)); }
    const auto rsW = MKRS(w2, F_ * D_ * 2);
    const int sW = w4 * 16 * 2048 + ct * 256 + s0 * (64 * 2048);
    const auto rsCr = MKRS(Cre, D_ * P_ * 2); const auto rsCi = MKRS(Cim, D_ * P_ * 2);
    const int sC = (ct * 128 + w4 * 32) * 2048;
#define BLDS2 BLDS
#define STAGE2(sl, so) do { LAS unsigned char* _d = lds + (so) + w4 * 4096; \
        _Pragma("unroll") for (int _i = 0; _i < 4; ++_i) BLDS2(rsA, _d + _i * 1024, voffL, sA + (sl) * 16384 + _i * 1024); \
        if (s0 + (sl) < 56) { _Pragma("unroll") for (int _i = 0; _i < 4; ++_i) BLDS2(rsW, _d + 16384 + _i * 1024, voffW[_i >> 1], sW + (sl) * (64 * 2048) + _i * 8192); } \
        else { const auto _r = (sl) < 28 ? rsCr : rsCi; \
               _Pragma("unroll") for (int _i = 0; _i < 4; ++_i) BLDS2(_r, _d + 16384 + _i * 1024, voffC[_i & 1], sC + (((sl) - 12) & 15) * 128 + _i * 16384); } } while (0)

    const unsigned lb = (unsigned)(size_t)lds;
    const int sw = (fq ^ (fr >> 1)) << 4;
    const unsigned aoff0 = lb + (wm * 64 + fr) * 128 + sw, aoff1 = lb + (wm * 64 + fr) * 128 + (sw ^ 64);
    const unsigned coff0 = lb + 16384 + (wn * 64 + fr) * 128 + sw, coff1 = lb + 16384 + (wn * 64 + fr) * 128 + (sw ^ 64);
    const int tq = (lane & 15) >> 2, tp = lane & 3, fl = tq | ((fq & 1) << 2);
    unsigned toff[4];
#pragma unroll
    for (int n = 0; n < 4; ++n) toff[n] = lb + 16384 + (8 * fq + tq) * 256 + (((wn * 4 + n) ^ fl) << 5) + 8 * tp;

    f32x4 acc[4][4];
#pragma unroll
    for (int m = 0; m < 4; ++m)
#pragma unroll
        for (int n = 0; n < 4; ++n) acc[m][n] = (f32x4){0.f, 0.f, 0.f, 0.f};
    bf16x8 Af[4][2], Bf[4][2]; s16x4 Bl[4][2], Bh[4][2];

    { LAS unsigned char* d0 = lds + w4 * 4096;
      if (grp == 0) { _Pragma("unroll") for (int i = 0; i < 4; ++i) BLDS2(rsA, d0 + i * 1024, voffL, sA + i * 1024); }
      else { _Pragma("unroll") for (int i = 0; i < 4; ++i) BLDS2(rsW, d0 + 16384 + i * 1024, voffW[i >> 1], sW + i * 8192); } }
    if (grp) STAGE2(2, 2 * K2_ST); else STAGE2(1, K2_ST);
    WAIT_V(8); BAR;
    if (grp) BAR;
    STAMP(1);
    int sl = grp, rdo = grp * K2_ST, sto = (grp + 3) * K2_ST;
#define K2_ADV do { sl += 2; rdo += 2 * K2_ST; if (rdo >= K2_LDS) rdo -= K2_LDS; sto += 2 * K2_ST; if (sto >= K2_LDS) sto -= K2_LDS; } while (0)
#define K2_MMA(BEXPR) do { __builtin_amdgcn_s_setprio(1); \
        _Pragma("unroll") for (int k = 0; k < 2; ++k) _Pragma("unroll") for (int m = 0; m < 4; ++m) _Pragma("unroll") for (int n = 0; n < 4; ++n) \
            acc[m][n] = __builtin_amdgcn_mfma_f32_16x16x32_bf16(BEXPR, Af[m][k], acc[m][n], 0, 0, 0); \
        __builtin_amdgcn_s_setprio(0); } while (0)
#define K2_DMA(TY, i) do { if ((TY) != 2) { LAS unsigned char* _d = lds + sto + w4 * 4096; \
        if ((i) < 4) BLDS2(rsA, _d + (i) * 1024, voffL, sA + (sl + 3) * 16384 + (i) * 1024); \
        else if ((TY) == 0) BLDS2(rsW, _d + 16384 + ((i) - 4) * 1024, voffW[((i) - 4) >> 1], sW + (sl + 3) * (64 * 2048) + ((i) - 4) * 8192); \
        else BLDS2(rsC, _d + 16384 + ((i) - 4) * 1024, voffC[(i) & 1], sC + ((sl + 3 - 12) & 15) * 128 + ((i) - 4) * 16384); } } while (0)
#define K2_STEP(RT, TY) do { \
        const unsigned a0 = aoff0 + rdo, a1 = aoff1 + rdo; const auto rsC = sl + 3 < 28 ? rsCr : rsCi; \
        if ((RT) == 0) { \
            _Pragma("unroll") for (int n = 0; n < 4; ++n) { const unsigned t = toff[n] + rdo; \
                K2_DMA(TY, 2 * n); DSRTR(Bl[n][0], t, 0); DSRTR(Bh[n][0], t, 1024); DSR128(Af[n][0], a0, n * 2048); \
                K2_DMA(TY, 2 * n + 1); DSRTR(Bl[n][1], t, 8192); DSRTR(Bh[n][1], t, 9216); DSR128(Af[n][1], a1, n * 2048); } \
        } else { const unsigned c0 = coff0 + rdo, c1 = coff1 + rdo; \
            _Pragma("unroll") for (int n = 0; n < 4; ++n) { \
                K2_DMA(TY, 2 * n); DSR128(Bf[n][0], c0, n * 2048); DSR128(Af[n][0], a0, n * 2048); \
                K2_DMA(TY, 2 * n + 1); DSR128(Bf[n][1], c1, n * 2048); DSR128(Af[n][1], a1, n * 2048); } } \
        if ((TY) != 2) WAIT_V(8); else WAIT_V(0); \
        BAR; WAIT_L(0); \
        asm volatile("" : "+v"(Af[0][0]), "+v"(Af[0][1]), "+v"(Af[1][0]), "+v"(Af[1][1]), "+v"(Af[2][0]), "+v"(Af[2][1]), "+v"(Af[3][0]), "+v"(Af[3][1])); \
        if ((RT) == 0) { \
            asm volatile("" : "+v"(Bl[0][0]), "+v"(Bh[0][0]), "+v"(Bl[1][0]), "+v"(Bh[1][0]), "+v"(Bl[2][0]), "+v"(Bh[2][0]), "+v"(Bl[3][0]), "+v"(Bh[3][0]), \
                              "+v"(Bl[0][1]), "+v"(Bh[0][1]), "+v"(Bl[1][1]), "+v"(Bh[1][1]), "+v"(Bl[2][1]), "+v"(Bh[2][1]), "+v"(Bl[3][1]), "+v"(Bh[3][1])); \
            SCHED; K2_MMA(CAT(Bl[n][k], Bh[n][k])); \
        } else { \
            asm volatile("" : "+v"(Bf[0][0]), "+v"(Bf[0][1]), "+v"(Bf[1][0]), "+v"(Bf[1][1]), "+v"(Bf[2][0]), "+v"(Bf[2][1]), "+v"(Bf[3][0]), "+v"(Bf[3][1])); \
            SCHED; K2_MMA(Bf[n][k]); } \
        SCHED; K2_ADV; BAR; SCHED; } while (0)
    if (kh == 0) {
        for (; sl < NS - 3;) K2_STEP(0, 0);
        for (; sl < NS;) K2_STEP(0, 2);
    } else {
        for (; sl < 9;) K2_STEP(0, 0);
        for (; sl < 12;) K2_STEP(0, 1);
        for (; sl < NS - 3;) K2_STEP(1, 1);
        for (; sl < NS;) K2_STEP(1, 2);
    }
    STAMP(2);
    if (grp == 0) BAR;
    STAMP(3);
    {
        LAS f32x4* xs = (LAS f32x4*)(lds + ((wid & 3) * 2 + grp) * 8192);
        const LAS f32x4* xr = (const LAS f32x4*)(lds + ((wid & 3) * 2 + (grp ^ 1)) * 8192);
        f32x4 r[2][4];
        if (grp) {
#pragma unroll
            for (int mm = 0; mm < 2; ++mm)
#pragma unroll
                for (int n = 0; n < 4; ++n) { xs[(mm * 4 + n) * 64 + lane] = acc[mm][n]; r[mm][n] = acc[2 + mm][n]; }
        } else {
#pragma unroll
            for (int mm = 0; mm < 2; ++mm)
#pragma unroll
                for (int n = 0; n < 4; ++n) { xs[(mm * 4 + n) * 64 + lane] = acc[2 + mm][n]; r[mm][n] = acc[mm][n]; }
        }
        const int mt = 2 * grp + kh;
        const int row = rt * 128 + wm * 64 + mt * 16 + fr;
        const float g = ((const float*)(ws + WS_G))[row];
        u32x2 xv[4], dv[4];
#pragma unroll
        for (int n = 0; n < 4; ++n) { const int col = ct * 128 + wn * 64 + n * 16 + fq * 4;
            xv[n] = *(const u32x2*)(x + (size_t)row * D_ + col); dv[n] = *(const u32x2*)(dsk + col); }
        WAIT_L(0); BAR;
#pragma unroll
        for (int mm = 0; mm < 2; ++mm)
#pragma unroll
            for (int n = 0; n < 4; ++n) r[mm][n] += xr[(mm * 4 + n) * 64 + lane];
        f32x4 mine[4], give[4];
        if (kh) {
#pragma unroll
            for (int n = 0; n < 4; ++n) { mine[n] = r[1][n]; give[n] = r[0][n]; }
        } else {
#pragma unroll
            for (int n = 0; n < 4; ++n) { mine[n] = r[0][n]; give[n] = r[1][n]; }
        }
        const int pbid = bid ^ 4;
        const auto rs = MKRS(ws + WS_XB, 256 * 32768);
#pragma unroll
        for (int n = 0; n < 4; ++n)
            __builtin_amdgcn_raw_buffer_store_b128(__builtin_bit_cast(u32x4, give[n]), rs, bid * 32768 + wid * 4096 + n * 1024 + lane * 16, 0, 16);
        STAMP(4);
#pragma unroll
        for (int n = 0; n < 4; ++n) {
            mine[n][0] += g * __uint_as_float(xv[n][0] << 16) * __uint_as_float(dv[n][0] << 16);
            mine[n][1] += g * __uint_as_float(xv[n][0] & 0xffff0000u) * __uint_as_float(dv[n][0] & 0xffff0000u);
            mine[n][2] += g * __uint_as_float(xv[n][1] << 16) * __uint_as_float(dv[n][1] << 16);
            mine[n][3] += g * __uint_as_float(xv[n][1] & 0xffff0000u) * __uint_as_float(dv[n][1] & 0xffff0000u); }
        u32x4 pv[4];
        const unsigned char* pp = ws + WS_XB + (size_t)pbid * 32768 + wid * 4096 + lane * 16;
        for (unsigned spins = 0;; ++spins) {
            asm volatile("global_load_dwordx4 %0, %4, off sc1\n\tglobal_load_dwordx4 %1, %4, off offset:1024 sc1\n\t"
                         "global_load_dwordx4 %2, %4, off offset:2048 sc1\n\tglobal_load_dwordx4 %3, %4, off offset:3072 sc1\n\ts_waitcnt vmcnt(0)"
                         : "=&v"(pv[0]), "=&v"(pv[1]), "=&v"(pv[2]), "=&v"(pv[3]) : "v"(pp) : "memory");
            bool ok = true;
#pragma unroll
            for (int n = 0; n < 4; ++n) ok &= (pv[n][0] != SENT) & (pv[n][1] != SENT) & (pv[n][2] != SENT) & (pv[n][3] != SENT);
            if (__all(ok) || spins > (1u << 18)) break;
            __builtin_amdgcn_s_sleep(1);
        }
        STAMP(5);
#pragma unroll
        for (int n = 0; n < 4; ++n)
            *(f32x4*)(out + (size_t)row * D_ + ct * 128 + wn * 64 + n * 16 + fq * 4) = mine[n] + __builtin_bit_cast(f32x4, pv[n]);
        STAMP(6);
        STAMP(7); STAMP_OUT(1);
    }
}
}

extern "C" void kernel_launch(void* const* d_in, const int* in_sizes, int n_in, void* d_out, int out_size, void* d_ws, size_t ws_size,
                              hipStream_t stream) {
    const u16* x = (const u16*)d_in[0]; const u16* lre = (const u16*)d_in[1]; const u16* lim = (const u16*)d_in[2];
    const u16* Bre = (const u16*)d_in[3]; const u16* Bim = (const u16*)d_in[4]; const u16* Cre = (const u16*)d_in[5]; const u16* Cim = (const u16*)d_in[6];
    const u16* ldt = (const u16*)d_in[7]; const u16* dsk = (const u16*)d_in[8];
    const u16* w1 = (const u16*)d_in[10]; const u16* w3 = (const u16*)d_in[11]; const u16* w2 = (const u16*)d_in[12]; const u16* gw = (const u16*)d_in[13];
    unsigned char* ws = (unsigned char*)d_ws;
    (void)hipFuncSetAttribute((const void*)k1_gemm, hipFuncAttributeMaxDynamicSharedMemorySize, K1_LDS);
    (void)hipFuncSetAttribute((const void*)k2_gemm, hipFuncAttributeMaxDynamicSharedMemorySize, K2_LDS);
    k0_prep<<<256, 512, 0, stream>>>(x, gw, lre, lim, ldt, w1, w3, Bre, Bim, ws);
    k1_gemm<<<256, 512, K1_LDS, stream>>>(x, w1, w3, Bre, Bim, w2, Cre, Cim, ws);
    k2_gemm<<<256, 512, K2_LDS, stream>>>(x, w2, Cre, Cim, dsk, ws, (float*)d_out);
}
```

```cpp
#include <hip/hip_runtime.h>
#include <hip/hip_bf16.h>
#include <stdint.h>

#define LAS __attribute__((address_space(3)))
#define GAS __attribute__((address_space(1)))
typedef unsigned short u16;
typedef unsigned long long u64;
typedef short bf16x8 __attribute__((ext_vector_type(8)));
typedef short s16x4 __attribute__((ext_vector_type(4)));
typedef float f32x4 __attribute__((ext_vector_type(4)));
typedef float f32x2 __attribute__((ext_vector_type(2)));
typedef unsigned u32x4 __attribute__((ext_vector_type(4)));
typedef unsigned u32x2 __attribute__((ext_vector_type(2)));

#define A2AUX 0
#define SSMAUX 16
namespace {
constexpr int S_ = 2048, D_ = 1024, P_ = 1024, F_ = 3584;
constexpr int KS2 = 88;
constexpr size_t WS_CNT = 0;
constexpr size_t WS_G = 262144;
constexpr size_t WS_AB = 262144 + 8192;
constexpr size_t WS_L256 = 262144 + 16384;
constexpr size_t WS_AGG = 524288;
constexpr size_t WS_A2 = 1u << 20;
constexpr size_t WS_XB = 32u << 20;
constexpr size_t WS_HL = 96u << 20;
constexpr unsigned SENT = 0xFFFFFFFFu;

__device__ __forceinline__ float bf2f(u16 v) { return __uint_as_float(((unsigned)v) << 16); }
__device__ __forceinline__ unsigned pk_bf16(float lo, float hi) {
    unsigned r; asm("v_cvt_pk_bf16_f32 %0, %1, %2" : "=v"(r) : "v"(lo), "v"(hi)); return r; }
__device__ __forceinline__ f32x2 cfma(f32x2 a, f32x2 b, f32x2 c) {
    const f32x2 t = __builtin_elementwise_fma((f32x2){a.x, a.x}, b, c);
    return __builtin_elementwise_fma((f32x2){-a.y, a.y}, (f32x2){b.y, b.x}, t); }
__device__ __forceinline__ f32x2 cmul(f32x2 a, f32x2 b) {
    const f32x2 t = (f32x2){a.x, a.x} * b;
    return __builtin_elementwise_fma((f32x2){-a.y, a.y}, (f32x2){b.y, b.x}, t); }

#define STAMP(i)
#define STAMP_DECL
#define STAMP_OUT(kern)
#define WAIT_V(n) asm volatile("s_waitcnt vmcnt(" #n ")" ::: "memory")
#define WAIT_L(n) asm volatile("s_waitcnt lgkmcnt(" #n ")" ::: "memory")
#define BAR __builtin_amdgcn_s_barrier()
#define SCHED __builtin_amdgcn_sched_barrier(0)
#define DSR128(dst, addr, off) asm volatile("ds_read_b128 %0, %1 offset:%2" : "=v"(dst) : "v"(addr), "i"(off))
#define DSRTR(dst, addr, off) asm volatile("ds_read_b64_tr_b16 %0, %1 offset:%2" : "=v"(dst) : "v"(addr), "i"(off))
#define CAT(lo, hi) __builtin_shufflevector(lo, hi, 0, 1, 2, 3, 4, 5, 6, 7)
#define BST(v, rs, voff, soff, aux) do { u32x4 _bv = (v); __builtin_amdgcn_raw_buffer_store_b128(_bv, (rs), (int)(voff), (int)(soff), (aux)); \
        asm volatile("s_nop 1" :: "v"(_bv)); } while (0)
#define MKRS(p, bytes) __builtin_amdgcn_make_buffer_rsrc((void*)(p), 0, (int)(bytes), 0x00020000)
#define BLDS(rs, dst, voff, soff) __builtin_amdgcn_raw_ptr_buffer_load_lds((rs), (LAS void*)(dst), 16, (int)(voff), (int)(soff), 0, 0)

constexpr int K1_A = 0, K1_W1 = 32768, K1_W3 = 32768 + 14336 + 128, K1_BS = K1_W3 + 14336, K1_BUF = K1_BS + 8192;
constexpr int K1_X0 = 2 * K1_BUF;
constexpr int K1_G = K1_X0, K1_CH = K1_X0 + 1024, K1_AGG = K1_X0 + 2048, K1_XC = K1_X0 + 3072, K1_PF = K1_X0 + 3328, K1_LDS = K1_X0 + 3840;
constexpr int RS = 368;

__device__ __forceinline__ size_t a2_addr(int R, int cf) {
    const int rt = R >> 7, rr = R & 127, s = cf >> 3, c = cf & 7;
    return ((size_t)(rt * KS2 + s) * 128 + rr) * 128 + ((c ^ ((rr >> 1) & 7)) << 4);
}

__global__ __launch_bounds__(512, 2) void k1_gemm(const u16* __restrict__ x, const u16* __restrict__ w1, const u16* __restrict__ w3,
                                                  const u16* __restrict__ Bre, const u16* __restrict__ Bim, const u16* __restrict__ w2,
                                                  const u16* __restrict__ Cre, const u16* __restrict__ Cim, const u16* __restrict__ gw,
                                                  const u16* __restrict__ lre, const u16* __restrict__ lim, const u16* __restrict__ ldt,
                                                  unsigned char* __restrict__ ws) {
    extern __shared__ __attribute__((aligned(16))) unsigned char smem[];
    LAS unsigned char* lds = (LAS unsigned char*)smem;
    const int tid = threadIdx.x, wid = __builtin_amdgcn_readfirstlane(tid >> 6), lane = tid & 63;
    const int wm = wid & 3, wn = wid >> 2, fr = lane & 15, fq = lane >> 4;
    const int bid = blockIdx.x, xcd = bid & 7, rr_ = bid >> 3, cg = xcd * 4 + (rr_ & 3), pm = rr_ >> 2;

    STAMP_DECL; STAMP(0);
    const auto rsX = MKRS(x, S_ * D_ * 2);
    const int sA = pm * 524288 + wid * 65536;
    unsigned voffA[2];
#pragma unroll
    for (int i = 0; i < 2; ++i) voffA[i] = (unsigned)((lane >> 3) * 2048 + (((lane & 7) ^ ((i * 4 + (lane >> 4)) & 7)) << 4));
    unsigned voffF[4]; int ldsF[4];
    const auto rsF0 = MKRS(w1, D_ * F_ * 2);
    const auto rsF1 = MKRS(wid + 8 < 14 ? w1 : w3, D_ * F_ * 2);
    const auto rsF2 = MKRS(w3, D_ * F_ * 2);
#pragma unroll
    for (int jj = 0; jj < 4; ++jj) {
        int j = wid + 8 * jj; if (j >= 28) j = 16 + wid;
        const int mat = j / 14, v = j % 14, idx = 64 * v + lane, k = idx / 14, pc = idx % 14;
        voffF[jj] = (unsigned)(k * 7168 + ((pc ^ ((k >> 3) & 1)) << 4));
        ldsF[jj] = (mat ? K1_W3 : K1_W1) + v * 1024;
    }
    const unsigned voffS = (unsigned)((8 * (wid & 1) + (lane >> 3)) * 2048 + (((lane & 7) ^ ((4 * (wid & 1) + (lane >> 4)) & 7)) << 4));
    const auto rsS = MKRS(((wid >> 1) & 1) ? Bim : Bre, P_ * D_ * 2);
    const int sS = (cg * 32 + 16 * (wid >> 2)) * 2048;

#define SA(c, par, t, i) do { if (c) BLDS(rsX, lds + (par) * K1_BUF + K1_A + (wid * 4 + (i)) * 1024, voffA[(i) & 1], sA + (t) * 128 + (i) * 16384); } while (0)
#define SB(c, par, t, j) do { if (c) { const int _sf = cg * 224 + (t) * (64 * 7168); \
        if ((j) == 0) BLDS(rsF0, lds + (par) * K1_BUF + ldsF[0], voffF[0], _sf); \
        if ((j) == 1) BLDS(rsF1, lds + (par) * K1_BUF + ldsF[1], voffF[1], _sf); \
        if ((j) == 2) BLDS(rsF2, lds + (par) * K1_BUF + ldsF[2], voffF[2], _sf); \
        if ((j) == 3) BLDS(rsF2, lds + (par) * K1_BUF + ldsF[3], voffF[3], _sf); \
        if ((j) == 4) BLDS(rsS, lds + (par) * K1_BUF + K1_BS + wid * 1024, voffS, sS + (t) * 128); } } while (0)
#define STAGE_A(par, t) do { _Pragma("unroll") for (int _i = 0; _i < 4; ++_i) \
        BLDS(rsX, lds + (par) * K1_BUF + K1_A + (wid * 4 + _i) * 1024, voffA[_i & 1], sA + (t) * 128 + _i * 16384); } while (0)
#define STAGE_B(par, t) do { const int _sf = cg * 224 + (t) * (64 * 7168); \
        BLDS(rsF0, lds + (par) * K1_BUF + ldsF[0], voffF[0], _sf); \
        BLDS(rsF1, lds + (par) * K1_BUF + ldsF[1], voffF[1], _sf); \
        BLDS(rsF2, lds + (par) * K1_BUF + ldsF[2], voffF[2], _sf); \
        if (wn == 0) BLDS(rsF2, lds + (par) * K1_BUF + ldsF[3], voffF[3], _sf); \
        BLDS(rsS, lds + (par) * K1_BUF + K1_BS + wid * 1024, voffS, sS + (t) * 128); } while (0)

    const u16* pfp; int pfs, pfk, pfb;
    if (bid < 32) { pfp = x; pfs = 11; pfk = 128; pfb = bid * 131072; }
    else if (bid < 144) { pfp = bid < 88 ? w1 : w3; pfs = 7; pfk = 64 * 7168; pfb = (bid < 88 ? bid - 32 : bid - 88) * 8192; }
    else { pfp = bid < 160 ? Bre : Bim; pfs = 11; pfk = 128; pfb = ((bid - 144) & 15) * 131072; }
    const auto rsXB = MKRS(ws + WS_XB, 256 * 32768);
#define PREFETCH(t) do { int _z = threadIdx.x; asm volatile("" : "+v"(_z)); _z &= 63; \
        if (wid == 4) { const auto _r = MKRS(pfp, D_ * F_ * 2); \
            __builtin_amdgcn_raw_ptr_buffer_load_lds(_r, (LAS void*)(lds + K1_PF), 4, _z << pfs, pfb + ((t) + 4) * pfk, 0, 0); \
        } else { const int _u = bid + 256 * (t); const auto _r = MKRS(_u < 896 ? w2 : (_u < 1152 ? Cre : Cim), F_ * D_ * 2); \
            __builtin_amdgcn_raw_ptr_buffer_load_lds(_r, (LAS void*)(lds + K1_PF + 256), 4, _z << 7, (_u < 896 ? _u : (_u - 896) & 255) * 8192, 0, 0); } } while (0)
    const unsigned lb = (unsigned)(size_t)lds;
    const int sw = fq ^ (fr >> 1);
    const int tq = (lane & 15) >> 2, tp = lane & 3;
    const int tbase = K1_W1 + (8 * fq + tq) * 224 + (tp & 1) * (K1_W3 - K1_W1) + 8 * (tp >> 1);
    unsigned aB[2][2], sB[2][2], tB[2][2];
#pragma unroll
    for (int p = 0; p < 2; ++p) {
        aB[p][0] = lb + p * K1_BUF + K1_A + (wm * 64 + fr) * 128 + (sw << 4); aB[p][1] = lb + p * K1_BUF + K1_A + (wm * 64 + fr) * 128 + ((sw ^ 4) << 4);
        sB[p][0] = lb + p * K1_BUF + K1_BS + fr * 128 + (sw << 4); sB[p][1] = lb + p * K1_BUF + K1_BS + fr * 128 + ((sw ^ 4) << 4);
        tB[p][0] = lb + p * K1_BUF + tbase + ((fq & 1) << 4); tB[p][1] = lb + p * K1_BUF + tbase + (((fq & 1) ^ 1) << 4);
    }

    f32x4 acc[4][9];
#pragma unroll
    for (int m = 0; m < 4; ++m)
#pragma unroll
        for (int n = 0; n < 9; ++n) acc[m][n] = (f32x4){0.f, 0.f, 0.f, 0.f};
    bf16x8 Af[4][2], Bs[3][2]; s16x4 Bl[3][2], Bh[3][2];

#define LDA1(par, m) do { DSR128(Af[m][0], aB[par][0], (m) * 2048); DSR128(Af[m][1], aB[par][1], (m) * 2048); } while (0)
#define LDA(par) do { _Pragma("unroll") for (int _m = 0; _m < 4; ++_m) { \
        DSR128(Af[_m][0], aB[par][0], _m * 2048); DSR128(Af[_m][1], aB[par][1], _m * 2048); } } while (0)
#define LDF(par, slot, i) do { \
        DSRTR(Bl[slot][0], tB[par][(i) & 1], ((i) >> 1) * 32); DSRTR(Bh[slot][0], tB[par][(i) & 1], 4 * 224 + ((i) >> 1) * 32); \
        DSRTR(Bl[slot][1], tB[par][(i) & 1], 32 * 224 + ((i) >> 1) * 32); DSRTR(Bh[slot][1], tB[par][(i) & 1], 36 * 224 + ((i) >> 1) * 32); } while (0)
#define LDS_(par, slot, tau) do { DSR128(Bs[slot][0], sB[par][0], (tau) * 2048); DSR128(Bs[slot][1], sB[par][1], (tau) * 2048); } while (0)
#define WA asm volatile("" : "+v"(Af[0][0]), "+v"(Af[0][1]), "+v"(Af[1][0]), "+v"(Af[1][1]), "+v"(Af[2][0]), "+v"(Af[2][1]), "+v"(Af[3][0]), "+v"(Af[3][1]))
#define WF(s) asm volatile("" : "+v"(Bl[s][0]), "+v"(Bh[s][0]), "+v"(Bl[s][1]), "+v"(Bh[s][1]))
#define WS_(s) asm volatile("" : "+v"(Bs[s][0]), "+v"(Bs[s][1]))
#define MMA3(n0, NS) do { __builtin_amdgcn_s_setprio(1); \
        _Pragma("unroll") for (int _k = 0; _k < 2; ++_k) _Pragma("unroll") for (int _m = 0; _m < 4; ++_m) _Pragma("unroll") for (int _n = 0; _n < 3; ++_n) { \
            if ((n0) + _n >= (NS)) acc[_m][(n0) + _n] = __builtin_amdgcn_mfma_f32_16x16x32_bf16(Af[_m][_k], Bs[_n][_k], acc[_m][(n0) + _n], 0, 0, 0); \
            else acc[_m][(n0) + _n] = __builtin_amdgcn_mfma_f32_16x16x32_bf16(CAT(Bl[_n][_k], Bh[_n][_k]), Af[_m][_k], acc[_m][(n0) + _n], 0, 0, 0); } \
        __builtin_amdgcn_s_setprio(0); } while (0)

    u16 pl = 0, pi = 0, pd = 0;
    if (wid == 1) { const int p = cg * 32 + (lane & 31); pl = lre[p]; pi = lim[p]; pd = ldt[p]; asm volatile("" : "+v"(pl), "+v"(pi), "+v"(pd)); }
    STAGE_A(0, 0); STAGE_B(0, 0); STAGE_A(1, 1);
    if (wid == 1 && lane < 32) {
        const float lr = bf2f(pl), li = bf2f(pi), dt = expf(bf2f(pd));
        const float er = expf(dt * lr); float sn, cs; sincosf(dt * li, &sn, &cs);
        const float br = er * cs, bi = er * sn;
        const float nr = br - 1.f, ni = bi, den = lr * lr + li * li;
        ((LAS f32x4*)(lds + K1_CH))[lane] = (f32x4){br, bi, (nr * lr + ni * li) / den, (ni * lr - nr * li) / den};
        if (pm == 0) ((f32x2*)(ws + WS_AB))[cg * 32 + lane] = (f32x2){dt * lr * 1.44269504089f, dt * li * 0.15915494309f};
    }
    if (bid == 0 && wid == 2 && lane < 8) ((unsigned*)(ws + WS_CNT))[lane * 16] = 0u;
    if (wid == 4 && bid < 176) { const auto r = MKRS(pfp, D_ * F_ * 2);
#pragma unroll
        for (int t = 1; t < 4; ++t) __builtin_amdgcn_raw_ptr_buffer_load_lds(r, (LAS void*)(lds + K1_PF), 4, lane << pfs, pfb + t * pfk, 0, 0);
        WAIT_V(7); } else if (wid == 1 || wid == 2) WAIT_V(0); else WAIT_V(4);
    WAIT_L(0); BAR;
    STAMP(1);
    if (wn == 0) {
#define STEP0(par, t, CB, CA) do { \
        LDA(par); LDF(par, 0, 0); LDF(par, 1, 1); LDF(par, 2, 2); \
        BAR; WAIT_L(0); WA; WF(0); WF(1); WF(2); SCHED; MMA3(0, 7); BAR; SCHED; \
        SB(CB, (par) ^ 1, (t) + 1, 0); SB(CB, (par) ^ 1, (t) + 1, 1); LDF(par, 0, 3); SB(CB, (par) ^ 1, (t) + 1, 2); LDF(par, 1, 4); SB(CB, (par) ^ 1, (t) + 1, 3); LDF(par, 2, 5); SB(CB, (par) ^ 1, (t) + 1, 4); \
        BAR; WAIT_L(0); WF(0); WF(1); WF(2); SCHED; MMA3(3, 7); BAR; SCHED; \
        SA(CA, par, (t) + 2, 0); LDF(par, 0, 6); SA(CA, par, (t) + 2, 1); LDS_(par, 1, 0); SA(CA, par, (t) + 2, 2); LDS_(par, 2, 1); SA(CA, par, (t) + 2, 3); \
        BAR; WAIT_L(0); WF(0); WS_(1); WS_(2); SCHED; MMA3(6, 7); if (CA) WAIT_V(4); else WAIT_V(0); BAR; SCHED; } while (0)
        for (int t = 0; t < 14; t += 2) { STEP0(0, t, true, true); STEP0(1, t + 1, true, true); }
        STEP0(0, 14, true, false); STEP0(1, 15, false, false);
    } else {
        BAR;
#define STEP1(par, t, CB, CA) do { \
        SB(CB, (par) ^ 1, (t) + 1, 0); LDA1(par, 0); LDF(par, 0, 7); SB(CB, (par) ^ 1, (t) + 1, 1); LDA1(par, 1); LDF(par, 1, 8); \
        SB(CB, (par) ^ 1, (t) + 1, 2); LDA1(par, 2); LDF(par, 2, 9); SB(CB, (par) ^ 1, (t) + 1, 4); LDA1(par, 3); \
        BAR; WAIT_L(0); WA; WF(0); WF(1); WF(2); SCHED; MMA3(0, 7); BAR; SCHED; \
        SA(CA, par, (t) + 2, 0); LDF(par, 0, 10); SA(CA, par, (t) + 2, 1); LDF(par, 1, 11); SA(CA, par, (t) + 2, 2); LDF(par, 2, 12); SA(CA, par, (t) + 2, 3); \
        BAR; WAIT_L(0); WF(0); WF(1); WF(2); SCHED; MMA3(3, 7); BAR; SCHED; \
        LDF(par, 0, 13); LDS_(par, 1, 2); LDS_(par, 2, 3); \
        if (wid >= 6) { int _z = threadIdx.x; asm volatile("" : "+v"(_z)); unsigned _s = SENT; asm volatile("" : "+v"(_s)); \
            BST(((u32x4){_s, _s, _s, _s}), rsXB, (_z & 127) * 16, bid * 32768 + (t) * 2048, 0); if (CA) WAIT_V(5); else WAIT_V(0); } \
        else if ((wid == 4 && (t) < 12 && bid < 176) || (wid == 5 && bid + 256 * (t) < 1408)) { PREFETCH(t); WAIT_V(5); } else if (CA) WAIT_V(4); else WAIT_V(0); \
        BAR; WAIT_L(0); WF(0); WS_(1); WS_(2); SCHED; MMA3(6, 7); if (CB) BAR; SCHED; } while (0)
        for (int t = 0; t < 14; t += 2) { STEP1(0, t, true, true); STEP1(1, t + 1, true, true); }
        STEP1(0, 14, true, false); STEP1(1, 15, false, false);
    }

    STAMP(2);
    {
    int tz = threadIdx.x; asm volatile("" : "+v"(tz));
    const int tid = tz, lane = tz & 63, fr = lane & 15, fq = lane >> 4;
    unsigned char* A2 = ws + WS_A2;
    const u32x4 gx0 = ((const u32x4*)(x + (size_t)(bid * 8 + wid) * D_))[lane], gx1 = ((const u32x4*)(x + (size_t)(bid * 8 + wid) * D_))[lane + 64];
    const u32x4 gw0 = ((const u32x4*)gw)[lane], gw1 = ((const u32x4*)gw)[lane + 64];
#define FFN_EPI(n, i) do { _Pragma("unroll") for (int _m = 0; _m < 4; ++_m) { \
        const f32x4 _v = acc[_m][n]; \
        const auto _s0 = __builtin_amdgcn_permlane16_swap(__float_as_uint(_v[0]), __float_as_uint(_v[2]), false, false); \
        const auto _s1 = __builtin_amdgcn_permlane16_swap(__float_as_uint(_v[1]), __float_as_uint(_v[3]), false, false); \
        const f32x2 _g = (f32x2){__uint_as_float(_s0[0]), __uint_as_float(_s1[0])}, _u = (f32x2){__uint_as_float(_s0[1]), __uint_as_float(_s1[1])}; \
        const f32x2 _t = _g * (-1.44269504089f); \
        const f32x2 _d = (f32x2){__builtin_amdgcn_exp2f(_t.x), __builtin_amdgcn_exp2f(_t.y)} + 1.0f; \
        const f32x2 _r = (f32x2){__builtin_amdgcn_rcpf(_d.x), __builtin_amdgcn_rcpf(_d.y)}; \
        const f32x2 _h = (_g * _u) * _r; \
        *(LAS unsigned*)(lds + (wm * 64 + _m * 16 + fr) * RS + (i) * 16 + fq * 4) = pk_bf16(_h.x, _h.y); } } while (0)

    const LAS f32x4* sch = (const LAS f32x4*)(lds + K1_CH);
    LAS f32x2* sagg = (LAS f32x2*)(lds + K1_AGG);
    f32x2 lam, l16, l64, lq, cf, ex[4], T[4];
    {
        const f32x4 c = sch[wn * 16 + fr];
        lam = (f32x2){c[0], c[1]}; cf = (f32x2){c[2], c[3]};
        const f32x2 l2 = cmul(lam, lam), l4 = cmul(l2, l2), l8 = cmul(l4, l4); l16 = cmul(l8, l8);
        const f32x2 l32 = cmul(l16, l16); l64 = cmul(l32, l32);
        const f32x2 l12 = cmul(l4, l8);
        lq = fq == 0 ? (f32x2){1.f, 0.f} : (fq == 1 ? l4 : (fq == 2 ? l8 : l12));
        f32x2 W = (f32x2){0.f, 0.f};
#pragma unroll
        for (int m = 0; m < 4; ++m) {
            f32x2 a = (f32x2){0.f, 0.f};
#pragma unroll
            for (int j = 0; j < 4; ++j) { const f32x2 u = cmul(cf, (f32x2){acc[m][7][j], acc[m][8][j]}); acc[m][7][j] = u.x; acc[m][8][j] = u.y; a = cfma(lam, a, u); }
            f32x2 x0, x1, x2, x3;
            { const auto p = __builtin_amdgcn_permlane16_swap(__float_as_uint(a.x), __float_as_uint(a.x), false, false);
              const auto q = __builtin_amdgcn_permlane32_swap(p[0], p[0], false, false), r = __builtin_amdgcn_permlane32_swap(p[1], p[1], false, false);
              x0.x = __uint_as_float(q[0]); x2.x = __uint_as_float(q[1]); x1.x = __uint_as_float(r[0]); x3.x = __uint_as_float(r[1]); }
            { const auto p = __builtin_amdgcn_permlane16_swap(__float_as_uint(a.y), __float_as_uint(a.y), false, false);
              const auto q = __builtin_amdgcn_permlane32_swap(p[0], p[0], false, false), r = __builtin_amdgcn_permlane32_swap(p[1], p[1], false, false);
              x0.y = __uint_as_float(q[0]); x2.y = __uint_as_float(q[1]); x1.y = __uint_as_float(r[0]); x3.y = __uint_as_float(r[1]); }
            const f32x2 e2 = cfma(l4, x0, x1), e3 = cfma(l4, e2, x2);
            T[m] = cfma(l4, e3, x3);
            ex[m] = fq == 0 ? (f32x2){0.f, 0.f} : (fq == 1 ? x0 : (fq == 2 ? e2 : e3));
            W = cfma(l16, W, T[m]);
        }
        if (fq == 0) sagg[wm * 32 + wn * 16 + fr] = W;
    }
    STAMP(3); WAIT_L(0); BAR;
    STAMP(4);
    const auto rsA2 = MKRS(A2, S_ * 5632 * 2); const auto rsHL = MKRS(ws + WS_HL, S_ * 5632 * 2);
    const int rtile = (pm * 2 + (wm >> 1)) * KS2;
    if (wid == 0 && lane < 32) {
        const f32x4 c = sch[lane];
        f32x2 p = (f32x2){c[0], c[1]};
#pragma unroll
        for (int i = 0; i < 6; ++i) p = cmul(p, p);
        f32x2 a = sagg[lane]; a = cfma(p, a, sagg[32 + lane]); a = cfma(p, a, sagg[64 + lane]); a = cfma(p, a, sagg[96 + lane]);
        ((f32x2*)(ws + WS_AGG))[pm * 1024 + cg * 32 + lane] = a;
        if (pm == 0) { p = cmul(p, p); ((f32x2*)(ws + WS_L256))[cg * 32 + lane] = cmul(p, p); }
    }
    {
        f32x2 c = (f32x2){0.f, 0.f};
        for (int w = 0; w < wm; ++w) c = cfma(l64, c, sagg[w * 32 + wn * 16 + fr]);
#pragma unroll
        for (int m = 0; m < 4; ++m) {
            f32x2 h = cfma(lq, c, ex[m]);
#pragma unroll
            for (int j = 0; j < 4; ++j) {
                h = cfma(lam, h, (f32x2){acc[m][7][j], acc[m][8][j]});
                const unsigned pk = pk_bf16(h.x, h.y);
                LAS unsigned char* dst = lds + (wm * 64 + m * 16 + fq * 4 + j) * RS + 224 + (wn * 16 + fr) * 2;
                *(LAS u16*)dst = (u16)(pk & 0xffffu);
                *(LAS u16*)(dst + 64) = (u16)(pk >> 16);
            }
            c = cfma(l16, c, T[m]);
        }
        const int pc = lane & 3, j = (pc >> 1) * 4 + wn * 2 + (pc & 1), cf = 448 + (pc >> 1) * 128 + cg * 4 + wn * 2 + (pc & 1), r0 = (wm & 1) * 64 + (lane >> 2);
        const unsigned go = (unsigned)(((cf >> 3) * 128 + r0) * 128 + (((cf & 7) ^ ((lane >> 3) & 7)) << 4));
        const unsigned lo = (unsigned)((wm * 64 + (lane >> 2)) * RS + 224 + j * 16);
        WAIT_L(0);
#pragma unroll
        for (int it = 0; it < 4; ++it) {
            const u32x4 v = *(const LAS u32x4*)(lds + lo + it * 16 * RS);
            BST(v, rsHL, go, rtile * 16384 + it * 2048, 0); }
    }
    STAMP(5);
#pragma unroll
    for (int n = 0; n < 7; ++n) FFN_EPI(n, wn * 7 + n);
    STAMP(6);
    {
        const int j = lane & 7, cf = cg * 14 + wn * 7 + j, r0 = (wm & 1) * 64 + (lane >> 3);
        const unsigned go = (unsigned)(((cf >> 3) * 128 + r0) * 128), cc = cf & 7, rs0 = (lane >> 4);
        const unsigned lo = (unsigned)((wm * 64 + (lane >> 3)) * RS + (wn * 7 + j) * 16);
        WAIT_L(0);
        if (j < 7) {
#pragma unroll
            for (int it = 0; it < 8; ++it) {
                const u32x4 v = *(const LAS u32x4*)(lds + lo + it * 8 * RS);
                BST(v, rsA2, go + ((cc ^ ((4 * it + rs0) & 7)) << 4), rtile * 16384 + it * 1024, 0); }
        }
    }
    {
        float sg = 0.f;
#pragma unroll
        for (int j = 0; j < 4; ++j) {
            sg += __uint_as_float(gx0[j] << 16) * __uint_as_float(gw0[j] << 16); sg += __uint_as_float(gx0[j] & 0xffff0000u) * __uint_as_float(gw0[j] & 0xffff0000u);
            sg += __uint_as_float(gx1[j] << 16) * __uint_as_float(gw1[j] << 16); sg += __uint_as_float(gx1[j] & 0xffff0000u) * __uint_as_float(gw1[j] & 0xffff0000u); }
#pragma unroll
        for (int o = 32; o >= 1; o >>= 1) sg += __shfl_xor(sg, o);
        if (lane == 0) ((float*)(ws + WS_G))[bid * 8 + wid] = 1.f / (1.f + __expf(-sg));
    }
    STAMP(9);
    STAMP(10); STAMP_OUT(0);
    }
}

constexpr int K2_ST = 32768, K2_NST = 5, K2_LDS = K2_NST * K2_ST;

__global__ __launch_bounds__(512, 2) void k2_gemm(const u16* __restrict__ x, const u16* __restrict__ w2, const u16* __restrict__ Cre,
                                                  const u16* __restrict__ Cim, const u16* __restrict__ dsk, unsigned char* __restrict__ ws,
                                                  float* __restrict__ out) {
    extern __shared__ __attribute__((aligned(16))) unsigned char smem[];
    LAS unsigned char* lds = (LAS unsigned char*)smem;
    const int tid = threadIdx.x, wid = __builtin_amdgcn_readfirstlane(tid >> 6), lane = tid & 63;
    const int grp = wid >> 2, wm = (wid >> 1) & 1, wn = wid & 1, fr = lane & 15, fq = lane >> 4;
    const int bid = blockIdx.x, xcd = bid & 7, r_ = bid >> 3, kh = xcd >> 2, sub = xcd & 3;
    const int rt = (sub >> 1) * 8 + (r_ >> 2), ct = (sub & 1) * 4 + (r_ & 3);
    const int s0 = kh * 44;
    constexpr int NS = 44;

    STAMP_DECL; STAMP(0);
    const int w4 = wid & 3;
    const auto rsA = MKRS(ws + WS_A2, S_ * 5632 * 2);
    const int sA = (rt * KS2 + s0) * 16384 + w4 * 4096;
    const unsigned voffL = lane * 16;
    unsigned voffW[2], voffC[2];
#pragma unroll
    for (int i = 0; i < 2; ++i) {
        voffW[i] = (unsigned)((lane >> 4) * 2048 + ((((lane & 15) >> 1) ^ ((lane >> 4) | (i << 2))) << 5) + ((lane & 1) << 4));
        voffC[i] = (unsigned)((lane >> 3) * 2048 + (((lane & 7) ^ ((i * 4 + (lane >> 4)) & 7)) << 4)); }
    const auto rsW = MKRS(w2, F_ * D_ * 2);
    const int sW = w4 * 16 * 2048 + ct * 256 + s0 * (64 * 2048);
    const auto rsCr = MKRS(Cre, D_ * P_ * 2); const auto rsCi = MKRS(Cim, D_ * P_ * 2);
    const int sC = (ct * 128 + w4 * 32) * 2048;
#define BLDS2 BLDS
#define STAGE2(sl, so) do { LAS unsigned char* _d = lds + (so) + w4 * 4096; \
        _Pragma("unroll") for (int _i = 0; _i < 4; ++_i) BLDS2(rsA, _d + _i * 1024, voffL, sA + (sl) * 16384 + _i * 1024); \
        if (s0 + (sl) < 56) { _Pragma("unroll") for (int _i = 0; _i < 4; ++_i) BLDS2(rsW, _d + 16384 + _i * 1024, voffW[_i >> 1], sW + (sl) * (64 * 2048) + _i * 8192); } \
        else { const auto _r = (sl) < 28 ? rsCr : rsCi; \
               _Pragma("unroll") for (int _i = 0; _i < 4; ++_i) BLDS2(_r, _d + 16384 + _i * 1024, voffC[_i & 1], sC + (((sl) - 12) & 15) * 128 + _i * 16384); } } while (0)

    const unsigned lb = (unsigned)(size_t)lds;
    const int sw = (fq ^ (fr >> 1)) << 4;
    const unsigned aoff0 = lb + (wm * 64 + fr) * 128 + sw, aoff1 = lb + (wm * 64 + fr) * 128 + (sw ^ 64);
    const unsigned coff0 = lb + 16384 + (wn * 64 + fr) * 128 + sw, coff1 = lb + 16384 + (wn * 64 + fr) * 128 + (sw ^ 64);
    const int tq = (lane & 15) >> 2, tp = lane & 3, fl = tq | ((fq & 1) << 2);
    unsigned toff[4];
#pragma unroll
    for (int n = 0; n < 4; ++n) toff[n] = lb + 16384 + (8 * fq + tq) * 256 + (((wn * 4 + n) ^ fl) << 5) + 8 * tp;

    f32x4 acc[4][4];
#pragma unroll
    for (int m = 0; m < 4; ++m)
#pragma unroll
        for (int n = 0; n < 4; ++n) acc[m][n] = (f32x4){0.f, 0.f, 0.f, 0.f};
    bf16x8 Af[4][2], Bf[4][2]; s16x4 Bl[4][2], Bh[4][2];

    const int fcg = xcd * 4 + (r_ & 3), fpm = r_ >> 2, cp = tid & 3, fro = tid >> 2;
    const float* gv = (const float*)(ws + WS_G);
    unsigned char* fa[2][2]; u32x4 fh[2][2]; f32x4 fab[4]; float fg[2], omg[4];
#pragma unroll
    for (int h = 0; h < 2; ++h)
#pragma unroll
        for (int k = 0; k < 2; ++k) { fa[h][k] = ws + WS_A2 + a2_addr(fpm * 256 + h * 128 + fro, 448 + k * 128 + fcg * 4 + cp); fh[h][k] = *(const u32x4*)(fa[h][k] + (WS_HL - WS_A2)); }
#pragma unroll
    for (int i = 0; i < 4; ++i) fab[i] = ((const f32x4*)(ws + WS_AB))[(fcg * 32 + cp * 8) / 2 + i];
    fg[0] = gv[fpm * 256 + fro]; fg[1] = gv[fpm * 256 + 128 + fro];
#pragma unroll
    for (int m = 0; m < 4; ++m) omg[m] = 1.f - gv[rt * 128 + wm * 64 + m * 16 + fr];
    const f32x2 aw = wid < 7 ? ((const f32x2*)(ws + WS_AGG))[wid * 1024 + fcg * 32 + (lane & 31)] : ((const f32x2*)(ws + WS_L256))[fcg * 32 + (lane & 31)];
    asm volatile("" ::: "memory");
    { LAS unsigned char* d0 = lds + w4 * 4096;
      if (grp == 0) { _Pragma("unroll") for (int i = 0; i < 4; ++i) BLDS2(rsA, d0 + i * 1024, voffL, sA + i * 1024); }
      else { _Pragma("unroll") for (int i = 0; i < 4; ++i) BLDS2(rsW, d0 + 16384 + i * 1024, voffW[i >> 1], sW + i * 8192); } }
    if (grp) STAGE2(2, 2 * K2_ST); else STAGE2(1, K2_ST);
    asm volatile("" ::: "memory");
    WAIT_V(8);
    { const unsigned xa = lb + 4 * K2_ST + 256 + wid * 256 + (lane & 31) * 8;
      asm volatile("ds_write_b64 %0, %1" :: "v"(xa), "v"(aw) : "memory"); }
    WAIT_L(0); BAR;
    if (wid == 0) {
        f32x2 aj[8], X = (f32x2){0.f, 0.f}; const unsigned xa = lb + 4 * K2_ST + (lane & 31) * 8;
#pragma unroll
        for (int j = 0; j < 8; ++j) asm volatile("ds_read_b64 %0, %1 offset:%2" : "=v"(aj[j]) : "v"(xa), "i"(256 + j * 256));
        WAIT_L(0);
        asm volatile("" : "+v"(aj[0]), "+v"(aj[1]), "+v"(aj[2]), "+v"(aj[3]), "+v"(aj[4]), "+v"(aj[5]), "+v"(aj[6]), "+v"(aj[7]));
#pragma unroll
        for (int j = 0; j < 7; ++j) if (j < fpm) X = cfma(aj[7], X, aj[j]);
        asm volatile("ds_write_b64 %0, %1" :: "v"(xa), "v"(X) : "memory");
    }
    WAIT_L(0); BAR;
    f32x4 fx[4];
    { const unsigned xa = lb + 4 * K2_ST + cp * 64;
#pragma unroll
      for (int i = 0; i < 4; ++i) asm volatile("ds_read_b128 %0, %1 offset:%2" : "=v"(fx[i]) : "v"(xa), "i"(i * 16)); }
    WAIT_L(0);
    asm volatile("" : "+v"(fx[0]), "+v"(fx[1]), "+v"(fx[2]), "+v"(fx[3]));
    BAR;
#define K2_FIXUP do { _Pragma("unroll") for (int h = 0; h < 2; ++h) { const float tau1 = (float)(h * 128 + fro + 1); u32x4 ore, oim; \
            _Pragma("unroll") for (int q = 0; q < 4; ++q) { f32x2 o[2]; \
                _Pragma("unroll") for (int e = 0; e < 2; ++e) { \
                    const float mag = __builtin_amdgcn_exp2f(tau1 * fab[q][2 * e]), ang = tau1 * fab[q][2 * e + 1]; \
                    const f32x2 pw = (f32x2){mag * __builtin_amdgcn_cosf(ang), mag * __builtin_amdgcn_sinf(ang)}; \
                    const f32x2 hl = (f32x2){__uint_as_float(e ? fh[h][0][q] & 0xffff0000u : fh[h][0][q] << 16), __uint_as_float(e ? fh[h][1][q] & 0xffff0000u : fh[h][1][q] << 16)}; \
                    o[e] = cfma(pw, (f32x2){fx[q][2 * e], fx[q][2 * e + 1]}, hl) * ((f32x2){2.f, -2.f} * fg[h]); } \
                ore[q] = pk_bf16(o[0].x, o[1].x); oim[q] = pk_bf16(o[0].y, o[1].y); } \
            asm volatile("global_store_dwordx4 %0, %1, off sc1\n\tglobal_store_dwordx4 %2, %3, off sc1\n\ts_nop 1" :: "v"(fa[h][0]), "v"(ore), "v"(fa[h][1]), "v"(oim) : "memory"); } } while (0)
    if (grp) BAR;
    STAMP(1);
    int sl = grp, rdo = grp * K2_ST, sto = (grp + 3) * K2_ST;
#define K2_ADV do { sl += 2; rdo += 2 * K2_ST; if (rdo >= K2_LDS) rdo -= K2_LDS; sto += 2 * K2_ST; if (sto >= K2_LDS) sto -= K2_LDS; } while (0)
#define K2_MMA(BEXPR) do { __builtin_amdgcn_s_setprio(1); \
        _Pragma("unroll") for (int k = 0; k < 2; ++k) _Pragma("unroll") for (int m = 0; m < 4; ++m) _Pragma("unroll") for (int n = 0; n < 4; ++n) \
            acc[m][n] = __builtin_amdgcn_mfma_f32_16x16x32_bf16(BEXPR, Af[m][k], acc[m][n], 0, 0, 0); \
        __builtin_amdgcn_s_setprio(0); } while (0)
#define K2_DMA(TY, i) do { if ((TY) != 2) { LAS unsigned char* _d = lds + sto + w4 * 4096; \
        if ((i) < 4) { if ((TY) == 1) __builtin_amdgcn_raw_ptr_buffer_load_lds(rsA, (LAS void*)(_d + (i) * 1024), 16, (int)voffL, sA + (sl + 3) * 16384 + (i) * 1024, 0, SSMAUX); \
            else BLDS2(rsA, _d + (i) * 1024, voffL, sA + (sl + 3) * 16384 + (i) * 1024); } \
        else if ((TY) == 0) BLDS2(rsW, _d + 16384 + ((i) - 4) * 1024, voffW[((i) - 4) >> 1], sW + (sl + 3) * (64 * 2048) + ((i) - 4) * 8192); \
        else BLDS2(rsC, _d + 16384 + ((i) - 4) * 1024, voffC[(i) & 1], sC + ((sl + 3 - 12) & 15) * 128 + ((i) - 4) * 16384); } } while (0)
#define K2_STEP(RT, TY) K2_STEPW(RT, TY, 8, false)
#define K2_STEPW(RT, TY, VW, FIX) do { \
        const unsigned a0 = aoff0 + rdo, a1 = aoff1 + rdo; const auto rsC = sl + 3 < 28 ? rsCr : rsCi; \
        if ((RT) == 0) { \
            _Pragma("unroll") for (int n = 0; n < 4; ++n) { const unsigned t = toff[n] + rdo; \
                K2_DMA(TY, 2 * n); DSRTR(Bl[n][0], t, 0); DSRTR(Bh[n][0], t, 1024); DSR128(Af[n][0], a0, n * 2048); \
                K2_DMA(TY, 2 * n + 1); DSRTR(Bl[n][1], t, 8192); DSRTR(Bh[n][1], t, 9216); DSR128(Af[n][1], a1, n * 2048); } \
        } else { const unsigned c0 = coff0 + rdo, c1 = coff1 + rdo; \
            _Pragma("unroll") for (int n = 0; n < 4; ++n) { \
                K2_DMA(TY, 2 * n); DSR128(Bf[n][0], c0, n * 2048); DSR128(Af[n][0], a0, n * 2048); \
                K2_DMA(TY, 2 * n + 1); DSR128(Bf[n][1], c1, n * 2048); DSR128(Af[n][1], a1, n * 2048); } } \
        if (FIX) K2_FIXUP; \
        if ((TY) != 2) WAIT_V(VW); else WAIT_V(0); \
        BAR; WAIT_L(0); \
        asm volatile("" : "+v"(Af[0][0]), "+v"(Af[0][1]), "+v"(Af[1][0]), "+v"(Af[1][1]), "+v"(Af[2][0]), "+v"(Af[2][1]), "+v"(Af[3][0]), "+v"(Af[3][1])); \
        if ((RT) == 0) { \
            asm volatile("" : "+v"(Bl[0][0]), "+v"(Bh[0][0]), "+v"(Bl[1][0]), "+v"(Bh[1][0]), "+v"(Bl[2][0]), "+v"(Bh[2][0]), "+v"(Bl[3][0]), "+v"(Bh[3][0]), \
                              "+v"(Bl[0][1]), "+v"(Bh[0][1]), "+v"(Bl[1][1]), "+v"(Bh[1][1]), "+v"(Bl[2][1]), "+v"(Bh[2][1]), "+v"(Bl[3][1]), "+v"(Bh[3][1])); \
            SCHED; K2_MMA(CAT(Bl[n][k], Bh[n][k])); \
        } else { \
            asm volatile("" : "+v"(Bf[0][0]), "+v"(Bf[0][1]), "+v"(Bf[1][0]), "+v"(Bf[1][1]), "+v"(Bf[2][0]), "+v"(Bf[2][1]), "+v"(Bf[3][0]), "+v"(Bf[3][1])); \
            SCHED; K2_MMA(Bf[n][k]); } \
        SCHED; K2_ADV; BAR; SCHED; } while (0)
#define K2_SCALE do { _Pragma("unroll") for (int m = 0; m < 4; ++m) _Pragma("unroll") for (int n = 0; n < 4; ++n) acc[m][n] *= omg[m]; } while (0)
    K2_STEPW(0, 0, 12, true);
    for (; sl < 6;) K2_STEP(0, 0);
    if (tid == 0) __hip_atomic_fetch_add((GAS unsigned*)(ws + WS_CNT) + fpm * 16, 1u, __ATOMIC_RELAXED, __HIP_MEMORY_SCOPE_AGENT);
    if (kh == 0) {
        for (; sl < NS - 3;) K2_STEP(0, 0);
        for (; sl < NS;) K2_STEP(0, 2);
        K2_SCALE;
    } else {
        for (; sl < 9;) K2_STEP(0, 0);
        {
            GAS unsigned* cnt = (GAS unsigned*)(ws + WS_CNT) + (rt >> 1) * 16;
            for (unsigned spins = 0; spins < (1u << 20); ++spins) {
                if ((unsigned)__builtin_amdgcn_readfirstlane(__hip_atomic_load(cnt, __ATOMIC_RELAXED, __HIP_MEMORY_SCOPE_AGENT)) >= 32u) break;
                __builtin_amdgcn_s_sleep(2); }
            asm volatile("" ::: "memory");
        }
        for (; sl < 12;) K2_STEP(0, 1);
        K2_SCALE;
        for (; sl < NS - 3;) K2_STEP(1, 1);
        for (; sl < NS;) K2_STEP(1, 2);
    }
    STAMP(2);
    if (grp == 0) BAR;
    STAMP(3);
    {
        LAS f32x4* xs = (LAS f32x4*)(lds + ((wid & 3) * 2 + grp) * 8192);
        const LAS f32x4* xr = (const LAS f32x4*)(lds + ((wid & 3) * 2 + (grp ^ 1)) * 8192);
        f32x4 r[2][4];
        if (grp) {
#pragma unroll
            for (int mm = 0; mm < 2; ++mm)
#pragma unroll
                for (int n = 0; n < 4; ++n) { xs[(mm * 4 + n) * 64 + lane] = acc[mm][n]; r[mm][n] = acc[2 + mm][n]; }
        } else {
#pragma unroll
            for (int mm = 0; mm < 2; ++mm)
#pragma unroll
                for (int n = 0; n < 4; ++n) { xs[(mm * 4 + n) * 64 + lane] = acc[2 + mm][n]; r[mm][n] = acc[mm][n]; }
        }
        const int mt = 2 * grp + kh;
        const int row = rt * 128 + wm * 64 + mt * 16 + fr;
        const float g = ((const float*)(ws + WS_G))[row];
        u32x2 xv[4], dv[4];
#pragma unroll
        for (int n = 0; n < 4; ++n) { const int col = ct * 128 + wn * 64 + n * 16 + fq * 4;
            xv[n] = *(const u32x2*)(x + (size_t)row * D_ + col); dv[n] = *(const u32x2*)(dsk + col); }
        WAIT_L(0); BAR;
#pragma unroll
        for (int mm = 0; mm < 2; ++mm)
#pragma unroll
            for (int n = 0; n < 4; ++n) r[mm][n] += xr[(mm * 4 + n) * 64 + lane];
        f32x4 mine[4], give[4];
        if (kh) {
#pragma unroll
            for (int n = 0; n < 4; ++n) { mine[n] = r[1][n]; give[n] = r[0][n]; }
        } else {
#pragma unroll
            for (int n = 0; n < 4; ++n) { mine[n] = r[0][n]; give[n] = r[1][n]; }
        }
        const int pbid = bid ^ 4;
        const auto rs = MKRS(ws + WS_XB, 256 * 32768);
#pragma unroll
        for (int n = 0; n < 4; ++n)
            BST(__builtin_bit_cast(u32x4, give[n]), rs, bid * 32768 + wid * 4096 + n * 1024 + lane * 16, 0, 16);
        STAMP(4);
#pragma unroll
        for (int n = 0; n < 4; ++n) {
            mine[n][0] += g * __uint_as_float(xv[n][0] << 16) * __uint_as_float(dv[n][0] << 16);
            mine[n][1] += g * __uint_as_float(xv[n][0] & 0xffff0000u) * __uint_as_float(dv[n][0] & 0xffff0000u);
            mine[n][2] += g * __uint_as_float(xv[n][1] << 16) * __uint_as_float(dv[n][1] << 16);
            mine[n][3] += g * __uint_as_float(xv[n][1] & 0xffff0000u) * __uint_as_float(dv[n][1] & 0xffff0000u); }
        u32x4 pv[4];
        const unsigned char* pp = ws + WS_XB + (size_t)pbid * 32768 + wid * 4096 + lane * 16;
        for (unsigned spins = 0;; ++spins) {
            asm volatile("global_load_dwordx4 %0, %4, off sc1\n\tglobal_load_dwordx4 %1, %4, off offset:1024 sc1\n\t"
                         "global_load_dwordx4 %2, %4, off offset:2048 sc1\n\tglobal_load_dwordx4 %3, %4, off offset:3072 sc1\n\ts_waitcnt vmcnt(0)"
                         : "=&v"(pv[0]), "=&v"(pv[1]), "=&v"(pv[2]), "=&v"(pv[3]) : "v"(pp) : "memory");
            bool ok = true;
#pragma unroll
            for (int n = 0; n < 4; ++n) ok &= (pv[n][0] != SENT) & (pv[n][1] != SENT) & (pv[n][2] != SENT) & (pv[n][3] != SENT);
            if (__all(ok) || spins > (1u << 18)) break;
            __builtin_amdgcn_s_sleep(1);
        }
        STAMP(5);
#pragma unroll
        for (int n = 0; n < 4; ++n)
            *(f32x4*)(out + (size_t)row * D_ + ct * 128 + wn * 64 + n * 16 + fq * 4) = mine[n] + __builtin_bit_cast(f32x4, pv[n]);
        STAMP(6);
        STAMP(7); STAMP_OUT(1);
    }
}
}

extern "C" void kernel_launch(void* const* d_in, const int* in_sizes, int n_in, void* d_out, int out_size, void* d_ws, size_t ws_size,
                              hipStream_t stream) {
    const u16* x = (const u16*)d_in[0]; const u16* lre = (const u16*)d_in[1]; const u16* lim = (const u16*)d_in[2];
    const u16* Bre = (const u16*)d_in[3]; const u16* Bim = (const u16*)d_in[4]; const u16* Cre = (const u16*)d_in[5]; const u16* Cim = (const u16*)d_in[6];
    const u16* ldt = (const u16*)d_in[7]; const u16* dsk = (const u16*)d_in[8];
    const u16* w1 = (const u16*)d_in[10]; const u16* w3 = (const u16*)d_in[11]; const u16* w2 = (const u16*)d_in[12]; const u16* gw = (const u16*)d_in[13];
    unsigned char* ws = (unsigned char*)d_ws;
    (void)hipFuncSetAttribute((const void*)k1_gemm, hipFuncAttributeMaxDynamicSharedMemorySize, K1_LDS);
    (void)hipFuncSetAttribute((const void*)k2_gemm, hipFuncAttributeMaxDynamicSharedMemorySize, K2_LDS);
    k1_gemm<<<256, 512, K1_LDS, stream>>>(x, w1, w3, Bre, Bim, w2, Cre, Cim, gw, lre, lim, ldt, ws);
    k2_gemm<<<256, 512, K2_LDS, stream>>>(x, w2, Cre, Cim, dsk, ws, (float*)d_out);
}
```

```cpp
#include <hip/hip_runtime.h>
#include <hip/hip_bf16.h>
#include <stdint.h>

#define LAS __attribute__((address_space(3)))
#define GAS __attribute__((address_space(1)))
typedef unsigned short u16;
typedef unsigned long long u64;
typedef short bf16x8 __attribute__((ext_vector_type(8)));
typedef short s16x4 __attribute__((ext_vector_type(4)));
typedef float f32x4 __attribute__((ext_vector_type(4)));
typedef float f32x2 __attribute__((ext_vector_type(2)));
typedef unsigned u32x4 __attribute__((ext_vector_type(4)));
typedef unsigned u32x2 __attribute__((ext_vector_type(2)));

namespace {
constexpr int S_ = 2048, D_ = 1024, P_ = 1024, F_ = 3584;
constexpr int KS2 = 88;
constexpr size_t WS_CNT = 0;
constexpr size_t WS_G = 262144;
constexpr size_t WS_AB = 262144 + 8192;
constexpr size_t WS_L256 = 262144 + 16384;
constexpr size_t WS_AGG = 524288;
constexpr size_t WS_A2 = 1u << 20;
constexpr size_t WS_XB = 32u << 20;
constexpr size_t WS_HL = 96u << 20;
constexpr unsigned SENT = 0xFFFFFFFFu;

__device__ __forceinline__ float bf2f(u16 v) { return __uint_as_float(((unsigned)v) << 16); }
__device__ __forceinline__ unsigned pk_bf16(float lo, float hi) {
    unsigned r; asm("v_cvt_pk_bf16_f32 %0, %1, %2" : "=v"(r) : "v"(lo), "v"(hi)); return r; }
__device__ __forceinline__ f32x2 cfma(f32x2 a, f32x2 b, f32x2 c) {
    const f32x2 t = __builtin_elementwise_fma((f32x2){a.x, a.x}, b, c);
    return __builtin_elementwise_fma((f32x2){-a.y, a.y}, (f32x2){b.y, b.x}, t); }
__device__ __forceinline__ f32x2 cmul(f32x2 a, f32x2 b) {
    const f32x2 t = (f32x2){a.x, a.x} * b;
    return __builtin_elementwise_fma((f32x2){-a.y, a.y}, (f32x2){b.y, b.x}, t); }

#define WAIT_V(n) asm volatile("s_waitcnt vmcnt(" #n ")" ::: "memory")
#define WAIT_L(n) asm volatile("s_waitcnt lgkmcnt(" #n ")" ::: "memory")
#define BAR __builtin_amdgcn_s_barrier()
#define SCHED __builtin_amdgcn_sched_barrier(0)
#define DSR128(dst, addr, off) asm volatile("ds_read_b128 %0, %1 offset:%2" : "=v"(dst) : "v"(addr), "i"(off))
#define DSRTR(dst, addr, off) asm volatile("ds_read_b64_tr_b16 %0, %1 offset:%2" : "=v"(dst) : "v"(addr), "i"(off))
#define CAT(lo, hi) __builtin_shufflevector(lo, hi, 0, 1, 2, 3, 4, 5, 6, 7)
#define BST(v, rs, voff, soff, aux) do { u32x4 _bv = (v); __builtin_amdgcn_raw_buffer_store_b128(_bv, (rs), (int)(voff), (int)(soff), (aux)); \
        asm volatile("s_nop 1" :: "v"(_bv)); } while (0)
#define MKRS(p, bytes) __builtin_amdgcn_make_buffer_rsrc((void*)(p), 0, (int)(bytes), 0x00020000)
#define BLDS(rs, dst, voff, soff) __builtin_amdgcn_raw_ptr_buffer_load_lds((rs), (LAS void*)(dst), 16, (int)(voff), (int)(soff), 0, 0)

constexpr int K1_A = 0, K1_W1 = 32768, K1_W3 = 32768 + 14336 + 128, K1_BS = K1_W3 + 14336, K1_BUF = K1_BS + 8192;
constexpr int K1_X0 = 2 * K1_BUF;
constexpr int K1_G = K1_X0, K1_CH = K1_X0 + 1024, K1_AGG = K1_X0 + 2048, K1_XC = K1_X0 + 3072, K1_PF = K1_X0 + 3328, K1_LDS = K1_X0 + 3840;
constexpr int RS = 368;

__device__ __forceinline__ size_t a2_addr(int R, int cf) {
    const int rt = R >> 7, rr = R & 127, s = cf >> 3, c = cf & 7;
    return ((size_t)(rt * KS2 + s) * 128 + rr) * 128 + ((c ^ ((rr >> 1) & 7)) << 4);
}

__global__ __launch_bounds__(512, 2) void k1_gemm(const u16* __restrict__ x, const u16* __restrict__ w1, const u16* __restrict__ w3,
                                                  const u16* __restrict__ Bre, const u16* __restrict__ Bim, const u16* __restrict__ w2,
                                                  const u16* __restrict__ Cre, const u16* __restrict__ Cim, const u16* __restrict__ gw,
                                                  const u16* __restrict__ lre, const u16* __restrict__ lim, const u16* __restrict__ ldt,
                                                  unsigned char* __restrict__ ws) {
    extern __shared__ __attribute__((aligned(16))) unsigned char smem[];
    LAS unsigned char* lds = (LAS unsigned char*)smem;
    const int tid = threadIdx.x, wid = __builtin_amdgcn_readfirstlane(tid >> 6), lane = tid & 63;
    const int wm = wid & 3, wn = wid >> 2, fr = lane & 15, fq = lane >> 4;
    const int bid = blockIdx.x, xcd = bid & 7, rr_ = bid >> 3, cg = xcd * 4 + (rr_ & 3), pm = rr_ >> 2;

    const auto rsX = MKRS(x, S_ * D_ * 2);
    const int sA = pm * 524288 + wid * 65536;
    unsigned voffA[2];
#pragma unroll
    for (int i = 0; i < 2; ++i) voffA[i] = (unsigned)((lane >> 3) * 2048 + (((lane & 7) ^ ((i * 4 + (lane >> 4)) & 7)) << 4));
    unsigned voffF[4]; int ldsF[4];
    const auto rsF0 = MKRS(w1, D_ * F_ * 2);
    const auto rsF1 = MKRS(wid + 8 < 14 ? w1 : w3, D_ * F_ * 2);
    const auto rsF2 = MKRS(w3, D_ * F_ * 2);
#pragma unroll
    for (int jj = 0; jj < 4; ++jj) {
        int j = wid + 8 * jj; if (j >= 28) j = 16 + wid;
        const int mat = j / 14, v = j % 14, idx = 64 * v + lane, k = idx / 14, pc = idx % 14;
        voffF[jj] = (unsigned)(k * 7168 + ((pc ^ ((k >> 3) & 1)) << 4));
        ldsF[jj] = (mat ? K1_W3 : K1_W1) + v * 1024;
    }
    const unsigned voffS = (unsigned)((8 * (wid & 1) + (lane >> 3)) * 2048 + (((lane & 7) ^ ((4 * (wid & 1) + (lane >> 4)) & 7)) << 4));
    const auto rsS = MKRS(((wid >> 1) & 1) ? Bim : Bre, P_ * D_ * 2);
    const int sS = (cg * 32 + 16 * (wid >> 2)) * 2048;

#define SA(c, par, t, i) do { if (c) BLDS(rsX, lds + (par) * K1_BUF + K1_A + (wid * 4 + (i)) * 1024, voffA[(i) & 1], sA + (t) * 128 + (i) * 16384); } while (0)
#define SB(c, par, t, j) do { if (c) { const int _sf = cg * 224 + (t) * (64 * 7168); \
        if ((j) == 0) BLDS(rsF0, lds + (par) * K1_BUF + ldsF[0], voffF[0], _sf); \
        if ((j) == 1) BLDS(rsF1, lds + (par) * K1_BUF + ldsF[1], voffF[1], _sf); \
        if ((j) == 2) BLDS(rsF2, lds + (par) * K1_BUF + ldsF[2], voffF[2], _sf); \
        if ((j) == 3) BLDS(rsF2, lds + (par) * K1_BUF + ldsF[3], voffF[3], _sf); \
        if ((j) == 4) BLDS(rsS, lds + (par) * K1_BUF + K1_BS + wid * 1024, voffS, sS + (t) * 128); } } while (0)
#define STAGE_A(par, t) do { _Pragma("unroll") for (int _i = 0; _i < 4; ++_i) \
        BLDS(rsX, lds + (par) * K1_BUF + K1_A + (wid * 4 + _i) * 1024, voffA[_i & 1], sA + (t) * 128 + _i * 16384); } while (0)
#define STAGE_B(par, t) do { const int _sf = cg * 224 + (t) * (64 * 7168); \
        BLDS(rsF0, lds + (par) * K1_BUF + ldsF[0], voffF[0], _sf); \
        BLDS(rsF1, lds + (par) * K1_BUF + ldsF[1], voffF[1], _sf); \
        BLDS(rsF2, lds + (par) * K1_BUF + ldsF[2], voffF[2], _sf); \
        if (wn == 0) BLDS(rsF2, lds + (par) * K1_BUF + ldsF[3], voffF[3], _sf); \
        BLDS(rsS, lds + (par) * K1_BUF + K1_BS + wid * 1024, voffS, sS + (t) * 128); } while (0)

    const u16* pfp; int pfs, pfk, pfb;
    if (bid < 32) { pfp = x; pfs = 11; pfk = 128; pfb = bid * 131072; }
    else if (bid < 144) { pfp = bid < 88 ? w1 : w3; pfs = 7; pfk = 64 * 7168; pfb = (bid < 88 ? bid - 32 : bid - 88) * 8192; }
    else { pfp = bid < 160 ? Bre : Bim; pfs = 11; pfk = 128; pfb = ((bid - 144) & 15) * 131072; }
    const auto rsXB = MKRS(ws + WS_XB, 256 * 32768);
#define PREFETCH(t) do { int _z = threadIdx.x; asm volatile("" : "+v"(_z)); _z &= 63; \
        if (wid == 4) { const auto _r = MKRS(pfp, D_ * F_ * 2); \
            __builtin_amdgcn_raw_ptr_buffer_load_lds(_r, (LAS void*)(lds + K1_PF), 4, _z << pfs, pfb + ((t) + 4) * pfk, 0, 0); \
        } else { const int _u = bid + 256 * (t); const auto _r = MKRS(_u < 896 ? w2 : (_u < 1152 ? Cre : Cim), F_ * D_ * 2); \
            __builtin_amdgcn_raw_ptr_buffer_load_lds(_r, (LAS void*)(lds + K1_PF + 256), 4, _z << 7, (_u < 896 ? _u : (_u - 896) & 255) * 8192, 0, 0); } } while (0)
    const unsigned lb = (unsigned)(size_t)lds;
    const int sw = fq ^ (fr >> 1);
    const int tq = (lane & 15) >> 2, tp = lane & 3;
    const int tbase = K1_W1 + (8 * fq + tq) * 224 + (tp & 1) * (K1_W3 - K1_W1) + 8 * (tp >> 1);
    unsigned aB[2][2], sB[2][2], tB[2][2];
#pragma unroll
    for (int p = 0; p < 2; ++p) {
        aB[p][0] = lb + p * K1_BUF + K1_A + (wm * 64 + fr) * 128 + (sw << 4); aB[p][1] = lb + p * K1_BUF + K1_A + (wm * 64 + fr) * 128 + ((sw ^ 4) << 4);
        sB[p][0] = lb + p * K1_BUF + K1_BS + fr * 128 + (sw << 4); sB[p][1] = lb + p * K1_BUF + K1_BS + fr * 128 + ((sw ^ 4) << 4);
        tB[p][0] = lb + p * K1_BUF + tbase + ((fq & 1) << 4); tB[p][1] = lb + p * K1_BUF + tbase + (((fq & 1) ^ 1) << 4);
    }

    f32x4 acc[4][9];
#pragma unroll
    for (int m = 0; m < 4; ++m)
#pragma unroll
        for (int n = 0; n < 9; ++n) acc[m][n] = (f32x4){0.f, 0.f, 0.f, 0.f};
    bf16x8 Af[4][2], Bs[3][2]; s16x4 Bl[3][2], Bh[3][2];

#define LDA1(par, m) do { DSR128(Af[m][0], aB[par][0], (m) * 2048); DSR128(Af[m][1], aB[par][1], (m) * 2048); } while (0)
#define LDA(par) do { _Pragma("unroll") for (int _m = 0; _m < 4; ++_m) { \
        DSR128(Af[_m][0], aB[par][0], _m * 2048); DSR128(Af[_m][1], aB[par][1], _m * 2048); } } while (0)
#define LDF(par, slot, i) do { \
        DSRTR(Bl[slot][0], tB[par][(i) & 1], ((i) >> 1) * 32); DSRTR(Bh[slot][0], tB[par][(i) & 1], 4 * 224 + ((i) >> 1) * 32); \
        DSRTR(Bl[slot][1], tB[par][(i) & 1], 32 * 224 + ((i) >> 1) * 32); DSRTR(Bh[slot][1], tB[par][(i) & 1], 36 * 224 + ((i) >> 1) * 32); } while (0)
#define LDS_(par, slot, tau) do { DSR128(Bs[slot][0], sB[par][0], (tau) * 2048); DSR128(Bs[slot][1], sB[par][1], (tau) * 2048); } while (0)
#define WA asm volatile("" : "+v"(Af[0][0]), "+v"(Af[0][1]), "+v"(Af[1][0]), "+v"(Af[1][1]), "+v"(Af[2][0]), "+v"(Af[2][1]), "+v"(Af[3][0]), "+v"(Af[3][1]))
#define WF(s) asm volatile("" : "+v"(Bl[s][0]), "+v"(Bh[s][0]), "+v"(Bl[s][1]), "+v"(Bh[s][1]))
#define WS_(s) asm volatile("" : "+v"(Bs[s][0]), "+v"(Bs[s][1]))
#define MMA3(n0, NS) do { __builtin_amdgcn_s_setprio(1); \
        _Pragma("unroll") for (int _k = 0; _k < 2; ++_k) _Pragma("unroll") for (int _m = 0; _m < 4; ++_m) _Pragma("unroll") for (int _n = 0; _n < 3; ++_n) { \
            if ((n0) + _n >= (NS)) acc[_m][(n0) + _n] = __builtin_amdgcn_mfma_f32_16x16x32_bf16(Af[_m][_k], Bs[_n][_k], acc[_m][(n0) + _n], 0, 0, 0); \
            else acc[_m][(n0) + _n] = __builtin_amdgcn_mfma_f32_16x16x32_bf16(CAT(Bl[_n][_k], Bh[_n][_k]), Af[_m][_k], acc[_m][(n0) + _n], 0, 0, 0); } \
        __builtin_amdgcn_s_setprio(0); } while (0)

    u16 pl = 0, pi = 0, pd = 0;
    if (wid == 1) { const int p = cg * 32 + (lane & 31); pl = lre[p]; pi = lim[p]; pd = ldt[p]; asm volatile("" : "+v"(pl), "+v"(pi), "+v"(pd)); }
    STAGE_A(0, 0); STAGE_B(0, 0); STAGE_A(1, 1);
    if (wid == 1 && lane < 32) {
        const float lr = bf2f(pl), li = bf2f(pi), dt = expf(bf2f(pd));
        const float er = expf(dt * lr); float sn, cs; sincosf(dt * li, &sn, &cs);
        const float br = er * cs, bi = er * sn;
        const float nr = br - 1.f, ni = bi, den = lr * lr + li * li;
        ((LAS f32x4*)(lds + K1_CH))[lane] = (f32x4){br, bi, (nr * lr + ni * li) / den, (ni * lr - nr * li) / den};
        if (pm == 0) ((f32x2*)(ws + WS_AB))[cg * 32 + lane] = (f32x2){dt * lr * 1.44269504089f, dt * li * 0.15915494309f};
    }
    if (bid == 0 && wid == 2 && lane < 8) ((unsigned*)(ws + WS_CNT))[lane * 16] = 0u;
    if (wid == 4 && bid < 176) { const auto r = MKRS(pfp, D_ * F_ * 2);
#pragma unroll
        for (int t = 1; t < 4; ++t) __builtin_amdgcn_raw_ptr_buffer_load_lds(r, (LAS void*)(lds + K1_PF), 4, lane << pfs, pfb + t * pfk, 0, 0);
        WAIT_V(7); } else if (wid == 1 || wid == 2) WAIT_V(0); else WAIT_V(4);
    WAIT_L(0); BAR;
    if (wn == 0) {
#define STEP0(par, t, CB, CA) do { \
        LDA(par); LDF(par, 0, 0); LDF(par, 1, 1); LDF(par, 2, 2); \
        BAR; WAIT_L(0); WA; WF(0); WF(1); WF(2); SCHED; MMA3(0, 7); BAR; SCHED; \
        SB(CB, (par) ^ 1, (t) + 1, 0); SB(CB, (par) ^ 1, (t) + 1, 1); LDF(par, 0, 3); SB(CB, (par) ^ 1, (t) + 1, 2); LDF(par, 1, 4); SB(CB, (par) ^ 1, (t) + 1, 3); LDF(par, 2, 5); SB(CB, (par) ^ 1, (t) + 1, 4); \
        BAR; WAIT_L(0); WF(0); WF(1); WF(2); SCHED; MMA3(3, 7); BAR; SCHED; \
        SA(CA, par, (t) + 2, 0); LDF(par, 0, 6); SA(CA, par, (t) + 2, 1); LDS_(par, 1, 0); SA(CA, par, (t) + 2, 2); LDS_(par, 2, 1); SA(CA, par, (t) + 2, 3); \
        BAR; WAIT_L(0); WF(0); WS_(1); WS_(2); SCHED; MMA3(6, 7); if (CA) WAIT_V(4); else WAIT_V(0); BAR; SCHED; } while (0)
        for (int t = 0; t < 14; t += 2) { STEP0(0, t, true, true); STEP0(1, t + 1, true, true); }
        STEP0(0, 14, true, false); STEP0(1, 15, false, false);
    } else {
        BAR;
#define STEP1(par, t, CB, CA) do { \
        SB(CB, (par) ^ 1, (t) + 1, 0); LDA1(par, 0); LDF(par, 0, 7); SB(CB, (par) ^ 1, (t) + 1, 1); LDA1(par, 1); LDF(par, 1, 8); \
        SB(CB, (par) ^ 1, (t) + 1, 2); LDA1(par, 2); LDF(par, 2, 9); SB(CB, (par) ^ 1, (t) + 1, 4); LDA1(par, 3); \
        BAR; WAIT_L(0); WA; WF(0); WF(1); WF(2); SCHED; MMA3(0, 7); BAR; SCHED; \
        SA(CA, par, (t) + 2, 0); LDF(par, 0, 10); SA(CA, par, (t) + 2, 1); LDF(par, 1, 11); SA(CA, par, (t) + 2, 2); LDF(par, 2, 12); SA(CA, par, (t) + 2, 3); \
        BAR; WAIT_L(0); WF(0); WF(1); WF(2); SCHED; MMA3(3, 7); BAR; SCHED; \
        LDF(par, 0, 13); LDS_(par, 1, 2); LDS_(par, 2, 3); \
        if (wid >= 6) { int _z = threadIdx.x; asm volatile("" : "+v"(_z)); unsigned _s = SENT; asm volatile("" : "+v"(_s)); \
            BST(((u32x4){_s, _s, _s, _s}), rsXB, (_z & 127) * 16, bid * 32768 + (t) * 2048, 0); if (CA) WAIT_V(5); else WAIT_V(0); } \
        else if ((wid == 4 && (t) < 12 && bid < 176) || (wid == 5 && bid + 256 * (t) < 1408)) { PREFETCH(t); WAIT_V(5); } else if (CA) WAIT_V(4); else WAIT_V(0); \
        BAR; WAIT_L(0); WF(0); WS_(1); WS_(2); SCHED; MMA3(6, 7); if (CB) BAR; SCHED; } while (0)
        for (int t = 0; t < 14; t += 2) { STEP1(0, t, true, true); STEP1(1, t + 1, true, true); }
        STEP1(0, 14, true, false); STEP1(1, 15, false, false);
    }

    {
    int tz = threadIdx.x; asm volatile("" : "+v"(tz));
    const int tid = tz, lane = tz & 63, fr = lane & 15, fq = lane >> 4;
    unsigned char* A2 = ws + WS_A2;
    const u32x4 gx0 = ((const u32x4*)(x + (size_t)(bid * 8 + wid) * D_))[lane], gx1 = ((const u32x4*)(x + (size_t)(bid * 8 + wid) * D_))[lane + 64];
    const u32x4 gw0 = ((const u32x4*)gw)[lane], gw1 = ((const u32x4*)gw)[lane + 64];
#define FFN_EPI(n, i) do { _Pragma("unroll") for (int _m = 0; _m < 4; ++_m) { \
        const f32x4 _v = acc[_m][n]; \
        const auto _s0 = __builtin_amdgcn_permlane16_swap(__float_as_uint(_v[0]), __float_as_uint(_v[2]), false, false); \
        const auto _s1 = __builtin_amdgcn_permlane16_swap(__float_as_uint(_v[1]), __float_as_uint(_v[3]), false, false); \
        const f32x2 _g = (f32x2){__uint_as_float(_s0[0]), __uint_as_float(_s1[0])}, _u = (f32x2){__uint_as_float(_s0[1]), __uint_as_float(_s1[1])}; \
        const f32x2 _t = _g * (-1.44269504089f); \
        const f32x2 _d = (f32x2){__builtin_amdgcn_exp2f(_t.x), __builtin_amdgcn_exp2f(_t.y)} + 1.0f; \
        const f32x2 _r = (f32x2){__builtin_amdgcn_rcpf(_d.x), __builtin_amdgcn_rcpf(_d.y)}; \
        const f32x2 _h = (_g * _u) * _r; \
        *(LAS unsigned*)(lds + (wm * 64 + _m * 16 + fr) * RS + (i) * 16 + fq * 4) = pk_bf16(_h.x, _h.y); } } while (0)

    const LAS f32x4* sch = (const LAS f32x4*)(lds + K1_CH);
    LAS f32x2* sagg = (LAS f32x2*)(lds + K1_AGG);
    f32x2 lam, l16, l64, lq, cf, ex[4], T[4];
    {
        const f32x4 c = sch[wn * 16 + fr];
        lam = (f32x2){c[0], c[1]}; cf = (f32x2){c[2], c[3]};
        const f32x2 l2 = cmul(lam, lam), l4 = cmul(l2, l2), l8 = cmul(l4, l4); l16 = cmul(l8, l8);
        const f32x2 l32 = cmul(l16, l16); l64 = cmul(l32, l32);
        const f32x2 l12 = cmul(l4, l8);
        lq = fq == 0 ? (f32x2){1.f, 0.f} : (fq == 1 ? l4 : (fq == 2 ? l8 : l12));
        f32x2 W = (f32x2){0.f, 0.f};
#pragma unroll
        for (int m = 0; m < 4; ++m) {
            f32x2 a = (f32x2){0.f, 0.f};
#pragma unroll
            for (int j = 0; j < 4; ++j) { const f32x2 u = cmul(cf, (f32x2){acc[m][7][j], acc[m][8][j]}); acc[m][7][j] = u.x; acc[m][8][j] = u.y; a = cfma(lam, a, u); }
            f32x2 x0, x1, x2, x3;
            { const auto p = __builtin_amdgcn_permlane16_swap(__float_as_uint(a.x), __float_as_uint(a.x), false, false);
              const auto q = __builtin_amdgcn_permlane32_swap(p[0], p[0], false, false), r = __builtin_amdgcn_permlane32_swap(p[1], p[1], false, false);
              x0.x = __uint_as_float(q[0]); x2.x = __uint_as_float(q[1]); x1.x = __uint_as_float(r[0]); x3.x = __uint_as_float(r[1]); }
            { const auto p = __builtin_amdgcn_permlane16_swap(__float_as_uint(a.y), __float_as_uint(a.y), false, false);
              const auto q = __builtin_amdgcn_permlane32_swap(p[0], p[0], false, false), r = __builtin_amdgcn_permlane32_swap(p[1], p[1], false, false);
              x0.y = __uint_as_float(q[0]); x2.y = __uint_as_float(q[1]); x1.y = __uint_as_float(r[0]); x3.y = __uint_as_float(r[1]); }
            const f32x2 e2 = cfma(l4, x0, x1), e3 = cfma(l4, e2, x2);
            T[m] = cfma(l4, e3, x3);
            ex[m] = fq == 0 ? (f32x2){0.f, 0.f} : (fq == 1 ? x0 : (fq == 2 ? e2 : e3));
            W = cfma(l16, W, T[m]);
        }
        if (fq == 0) sagg[wm * 32 + wn * 16 + fr] = W;
    }
    WAIT_L(0); BAR;
    const auto rsA2 = MKRS(A2, S_ * 5632 * 2); const auto rsHL = MKRS(ws + WS_HL, S_ * 5632 * 2);
    const int rtile = (pm * 2 + (wm >> 1)) * KS2;
    if (wid == 0 && lane < 32) {
        const f32x4 c = sch[lane];
        f32x2 p = (f32x2){c[0], c[1]};
#pragma unroll
        for (int i = 0; i < 6; ++i) p = cmul(p, p);
        f32x2 a = sagg[lane]; a = cfma(p, a, sagg[32 + lane]); a = cfma(p, a, sagg[64 + lane]); a = cfma(p, a, sagg[96 + lane]);
        ((f32x2*)(ws + WS_AGG))[pm * 1024 + cg * 32 + lane] = a;
        if (pm == 0) { p = cmul(p, p); ((f32x2*)(ws + WS_L256))[cg * 32 + lane] = cmul(p, p); }
    }
    {
        f32x2 c = (f32x2){0.f, 0.f};
        for (int w = 0; w < wm; ++w) c = cfma(l64, c, sagg[w * 32 + wn * 16 + fr]);
#pragma unroll
        for (int m = 0; m < 4; ++m) {
            f32x2 h = cfma(lq, c, ex[m]);
#pragma unroll
            for (int j = 0; j < 4; ++j) {
                h = cfma(lam, h, (f32x2){acc[m][7][j], acc[m][8][j]});
                const unsigned pk = pk_bf16(h.x, h.y);
                LAS unsigned char* dst = lds + (wm * 64 + m * 16 + fq * 4 + j) * RS + 224 + (wn * 16 + fr) * 2;
                *(LAS u16*)dst = (u16)(pk & 0xffffu);
                *(LAS u16*)(dst + 64) = (u16)(pk >> 16);
            }
            c = cfma(l16, c, T[m]);
        }
        const int pc = lane & 3, j = (pc >> 1) * 4 + wn * 2 + (pc & 1), cf = 448 + (pc >> 1) * 128 + cg * 4 + wn * 2 + (pc & 1), r0 = (wm & 1) * 64 + (lane >> 2);
        const unsigned go = (unsigned)(((cf >> 3) * 128 + r0) * 128 + (((cf & 7) ^ ((lane >> 3) & 7)) << 4));
        const unsigned lo = (unsigned)((wm * 64 + (lane >> 2)) * RS + 224 + j * 16);
        WAIT_L(0);
#pragma unroll
        for (int it = 0; it < 4; ++it) {
            const u32x4 v = *(const LAS u32x4*)(lds + lo + it * 16 * RS);
            BST(v, rsHL, go, rtile * 16384 + it * 2048, 0); }
    }
#pragma unroll
    for (int n = 0; n < 7; ++n) FFN_EPI(n, wn * 7 + n);
    {
        const int j = lane & 7, cf = cg * 14 + wn * 7 + j, r0 = (wm & 1) * 64 + (lane >> 3);
        const unsigned go = (unsigned)(((cf >> 3) * 128 + r0) * 128), cc = cf & 7, rs0 = (lane >> 4);
        const unsigned lo = (unsigned)((wm * 64 + (lane >> 3)) * RS + (wn * 7 + j) * 16);
        WAIT_L(0);
        if (j < 7) {
#pragma unroll
            for (int it = 0; it < 8; ++it) {
                const u32x4 v = *(const LAS u32x4*)(lds + lo + it * 8 * RS);
                BST(v, rsA2, go + ((cc ^ ((4 * it + rs0) & 7)) << 4), rtile * 16384 + it * 1024, 0); }
        }
    }
    {
        float sg = 0.f;
#pragma unroll
        for (int j = 0; j < 4; ++j) {
            sg += __uint_as_float(gx0[j] << 16) * __uint_as_float(gw0[j] << 16); sg += __uint_as_float(gx0[j] & 0xffff0000u) * __uint_as_float(gw0[j] & 0xffff0000u);
            sg += __uint_as_float(gx1[j] << 16) * __uint_as_float(gw1[j] << 16); sg += __uint_as_float(gx1[j] & 0xffff0000u) * __uint_as_float(gw1[j] & 0xffff0000u); }
#pragma unroll
        for (int o = 32; o >= 1; o >>= 1) sg += __shfl_xor(sg, o);
        if (lane == 0) ((float*)(ws + WS_G))[bid * 8 + wid] = 1.f / (1.f + __expf(-sg));
    }

    }
}

constexpr int K2_ST = 32768, K2_NST = 5, K2_LDS = K2_NST * K2_ST;

__global__ __launch_bounds__(512, 2) void k2_gemm(const u16* __restrict__ x, const u16* __restrict__ w2, const u16* __restrict__ Cre,
                                                  const u16* __restrict__ Cim, const u16* __restrict__ dsk, unsigned char* __restrict__ ws,
                                                  float* __restrict__ out) {
    extern __shared__ __attribute__((aligned(16))) unsigned char smem[];
    LAS unsigned char* lds = (LAS unsigned char*)smem;
    const int tid = threadIdx.x, wid = __builtin_amdgcn_readfirstlane(tid >> 6), lane = tid & 63;
    const int grp = wid >> 2, wm = (wid >> 1) & 1, wn = wid & 1, fr = lane & 15, fq = lane >> 4;
    const int bid = blockIdx.x, xcd = bid & 7, r_ = bid >> 3, kh = xcd >> 2, sub = xcd & 3;
    const int rt = (sub >> 1) * 8 + (r_ >> 2), ct = (sub & 1) * 4 + (r_ & 3);
    constexpr int NS = 44, NF = 28;

    const int w4 = wid & 3;
    const auto rsA = MKRS(ws + WS_A2, S_ * 5632 * 2);
    const int sA = (rt * KS2 + kh * NF) * 16384 + w4 * 4096, sA2 = (rt * KS2 + 56 + kh * 16 - NF) * 16384 + w4 * 4096;
    const unsigned voffL = lane * 16;
    unsigned voffW[2], voffC[2];
#pragma unroll
    for (int i = 0; i < 2; ++i) {
        voffW[i] = (unsigned)((lane >> 4) * 2048 + ((((lane & 15) >> 1) ^ ((lane >> 4) | (i << 2))) << 5) + ((lane & 1) << 4));
        voffC[i] = (unsigned)((lane >> 3) * 2048 + (((lane & 7) ^ ((i * 4 + (lane >> 4)) & 7)) << 4)); }
    const auto rsW = MKRS(w2, F_ * D_ * 2);
    const int sW = w4 * 16 * 2048 + ct * 256 + kh * NF * (64 * 2048);
    const auto rsC = MKRS(kh ? Cim : Cre, D_ * P_ * 2);
    const int sC = (ct * 128 + w4 * 32) * 2048;
#define STAGE2(sl, so) do { LAS unsigned char* _d = lds + (so) + w4 * 4096; \
        _Pragma("unroll") for (int _i = 0; _i < 4; ++_i) BLDS(rsA, _d + _i * 1024, voffL, sA + (sl) * 16384 + _i * 1024); \
        _Pragma("unroll") for (int _i = 0; _i < 4; ++_i) BLDS(rsW, _d + 16384 + _i * 1024, voffW[_i >> 1], sW + (sl) * (64 * 2048) + _i * 8192); } while (0)

    const unsigned lb = (unsigned)(size_t)lds;
    const int sw = (fq ^ (fr >> 1)) << 4;
    const unsigned aoff0 = lb + (wm * 64 + fr) * 128 + sw, aoff1 = lb + (wm * 64 + fr) * 128 + (sw ^ 64);
    const unsigned coff0 = lb + 16384 + (wn * 64 + fr) * 128 + sw, coff1 = lb + 16384 + (wn * 64 + fr) * 128 + (sw ^ 64);
    const int tq = (lane & 15) >> 2, tp = lane & 3, fl = tq | ((fq & 1) << 2);
    unsigned toff[4];
#pragma unroll
    for (int n = 0; n < 4; ++n) toff[n] = lb + 16384 + (8 * fq + tq) * 256 + (((wn * 4 + n) ^ fl) << 5) + 8 * tp;

    f32x4 acc[4][4];
#pragma unroll
    for (int m = 0; m < 4; ++m)
#pragma unroll
        for (int n = 0; n < 4; ++n) acc[m][n] = (f32x4){0.f, 0.f, 0.f, 0.f};
    bf16x8 Af[4][2], Bf[4][2]; s16x4 Bl[4][2], Bh[4][2];

    const int fcg = xcd * 4 + (r_ & 3), fpm = r_ >> 2, cp = tid & 3, fro = tid >> 2;
    unsigned char* fa[2][2]; u32x4 fh[2][2]; f32x4 fab[4]; float fg[2], omg[4]; f32x2 aw;
#define GLD(ty, dst, p) asm volatile("global_load_" ty " %0, %1, off" : "=v"(dst) : "v"(p) : "memory")
    GLD("dwordx2", aw, ws + (wid < 7 ? WS_AGG + wid * 8192 : WS_L256) + (fcg * 32 + (lane & 31)) * 8);
    { LAS unsigned char* d0 = lds + w4 * 4096;
      if (grp == 0) { _Pragma("unroll") for (int i = 0; i < 4; ++i) BLDS(rsA, d0 + i * 1024, voffL, sA + i * 1024); }
      else { _Pragma("unroll") for (int i = 0; i < 4; ++i) BLDS(rsW, d0 + 16384 + i * 1024, voffW[i >> 1], sW + i * 8192); } }
    if (grp) STAGE2(2, 2 * K2_ST); else STAGE2(1, K2_ST);
#pragma unroll
    for (int h = 0; h < 2; ++h)
#pragma unroll
        for (int k = 0; k < 2; ++k) fa[h][k] = ws + WS_A2 + a2_addr(fpm * 256 + h * 128 + fro, 448 + k * 128 + fcg * 4 + cp);
#define K2_FIXLD do { \
        _Pragma("unroll") for (int h = 0; h < 2; ++h) _Pragma("unroll") for (int k = 0; k < 2; ++k) GLD("dwordx4", fh[h][k], fa[h][k] + (WS_HL - WS_A2)); \
        _Pragma("unroll") for (int i = 0; i < 4; ++i) GLD("dwordx4", fab[i], ws + WS_AB + (fcg * 32 + cp * 8) * 8 + i * 16); \
        _Pragma("unroll") for (int h = 0; h < 2; ++h) GLD("dword", fg[h], ws + WS_G + (fpm * 256 + h * 128 + fro) * 4); \
        _Pragma("unroll") for (int m = 0; m < 4; ++m) GLD("dword", omg[m], ws + WS_G + (rt * 128 + wm * 64 + m * 16 + fr) * 4); } while (0)
    WAIT_V(8);
    asm volatile("" : "+v"(aw));
    { const unsigned xa = lb + 4 * K2_ST + 256 + wid * 256 + (lane & 31) * 8;
      asm volatile("ds_write_b64 %0, %1" :: "v"(xa), "v"(aw) : "memory"); }
    WAIT_L(0); BAR;
    if (wid == 0) {
        f32x2 aj[8], X = (f32x2){0.f, 0.f}; const unsigned xa = lb + 4 * K2_ST + (lane & 31) * 8;
#pragma unroll
        for (int j = 0; j < 8; ++j) asm volatile("ds_read_b64 %0, %1 offset:%2" : "=v"(aj[j]) : "v"(xa), "i"(256 + j * 256));
        WAIT_L(0);
        asm volatile("" : "+v"(aj[0]), "+v"(aj[1]), "+v"(aj[2]), "+v"(aj[3]), "+v"(aj[4]), "+v"(aj[5]), "+v"(aj[6]), "+v"(aj[7]));
#pragma unroll
        for (int j = 0; j < 7; ++j) if (j < fpm) X = cfma(aj[7], X, aj[j]);
        asm volatile("ds_write_b64 %0, %1" :: "v"(xa), "v"(X) : "memory");
    }
    WAIT_L(0); BAR;
    f32x4 fx[4];
    { const unsigned xa = lb + 4 * K2_ST + cp * 64;
#pragma unroll
      for (int i = 0; i < 4; ++i) asm volatile("ds_read_b128 %0, %1 offset:%2" : "=v"(fx[i]) : "v"(xa), "i"(i * 16)); }
    WAIT_L(0);
    asm volatile("" : "+v"(fx[0]), "+v"(fx[1]), "+v"(fx[2]), "+v"(fx[3]));
    BAR;
#define K2_FIXMATH(h) do { const float tau1 = (float)((h) * 128 + fro + 1); \
            _Pragma("unroll") for (int q = 0; q < 4; ++q) { f32x2 o[2]; \
                _Pragma("unroll") for (int e = 0; e < 2; ++e) { \
                    const float mag = __builtin_amdgcn_exp2f(tau1 * fab[q][2 * e]), ang = __builtin_amdgcn_fractf(tau1 * fab[q][2 * e + 1]); \
                    const f32x2 pw = (f32x2){mag * __builtin_amdgcn_cosf(ang), mag * __builtin_amdgcn_sinf(ang)}; \
                    const f32x2 hl = (f32x2){__uint_as_float(e ? fh[h][0][q] & 0xffff0000u : fh[h][0][q] << 16), __uint_as_float(e ? fh[h][1][q] & 0xffff0000u : fh[h][1][q] << 16)}; \
                    o[e] = cfma(pw, (f32x2){fx[q][2 * e], fx[q][2 * e + 1]}, hl) * ((f32x2){2.f, -2.f} * fg[h]); } \
                ore[q] = pk_bf16(o[0].x, o[1].x); oim[q] = pk_bf16(o[0].y, o[1].y); } } while (0)
#define K2_FIXST(h) asm volatile("global_store_dwordx4 %0, %1, off sc1\n\tglobal_store_dwordx4 %2, %3, off sc1\n\ts_nop 1" :: "v"(fa[h][0]), "v"(ore), "v"(fa[h][1]), "v"(oim) : "memory")
#define K2_FIXPIN asm volatile("" : "+v"(fh[0][0]), "+v"(fh[0][1]), "+v"(fh[1][0]), "+v"(fh[1][1]), "+v"(fab[0]), "+v"(fab[1]), "+v"(fab[2]), "+v"(fab[3]), \
                          "+v"(fg[0]), "+v"(fg[1]), "+v"(omg[0]), "+v"(omg[1]), "+v"(omg[2]), "+v"(omg[3]))
    if (grp) BAR;
    int sl = grp, rdo = grp * K2_ST, sto = (grp + 3) * K2_ST;
#define K2_ADV do { sl += 2; rdo += 2 * K2_ST; if (rdo >= K2_LDS) rdo -= K2_LDS; sto += 2 * K2_ST; if (sto >= K2_LDS) sto -= K2_LDS; } while (0)
#define K2_MMA(BEXPR) do { __builtin_amdgcn_s_setprio(1); \
        _Pragma("unroll") for (int k = 0; k < 2; ++k) _Pragma("unroll") for (int m = 0; m < 4; ++m) _Pragma("unroll") for (int n = 0; n < 4; ++n) \
            acc[m][n] = __builtin_amdgcn_mfma_f32_16x16x32_bf16(BEXPR, Af[m][k], acc[m][n], 0, 0, 0); \
        __builtin_amdgcn_s_setprio(0); } while (0)
#define K2_DMA(TY, i) do { if ((TY) != 2) { LAS unsigned char* _d = lds + sto + w4 * 4096; \
        if ((i) < 4) { if ((TY) == 1) __builtin_amdgcn_raw_ptr_buffer_load_lds(rsA, (LAS void*)(_d + (i) * 1024), 16, (int)voffL, sA2 + (sl + 3) * 16384 + (i) * 1024, 0, 16); \
            else BLDS(rsA, _d + (i) * 1024, voffL, sA + (sl + 3) * 16384 + (i) * 1024); } \
        else if ((TY) == 0) BLDS(rsW, _d + 16384 + ((i) - 4) * 1024, voffW[((i) - 4) >> 1], sW + (sl + 3) * (64 * 2048) + ((i) - 4) * 8192); \
        else BLDS(rsC, _d + 16384 + ((i) - 4) * 1024, voffC[(i) & 1], sC + (sl + 3 - NF) * 128 + ((i) - 4) * 16384); } } while (0)
#define K2_STEP(RT, TY) K2_STEPW(RT, TY, 8, 0)
#define K2_STEPW(RT, TY, VW, FIX) do { \
        const unsigned a0 = aoff0 + rdo, a1 = aoff1 + rdo; \
        if ((RT) == 0) { \
            _Pragma("unroll") for (int n = 0; n < 4; ++n) { const unsigned t = toff[n] + rdo; \
                K2_DMA(TY, 2 * n); DSRTR(Bl[n][0], t, 0); DSRTR(Bh[n][0], t, 1024); DSR128(Af[n][0], a0, n * 2048); \
                K2_DMA(TY, 2 * n + 1); DSRTR(Bl[n][1], t, 8192); DSRTR(Bh[n][1], t, 9216); DSR128(Af[n][1], a1, n * 2048); } \
        } else { const unsigned c0 = coff0 + rdo, c1 = coff1 + rdo; \
            _Pragma("unroll") for (int n = 0; n < 4; ++n) { \
                K2_DMA(TY, 2 * n); DSR128(Bf[n][0], c0, n * 2048); DSR128(Af[n][0], a0, n * 2048); \
                K2_DMA(TY, 2 * n + 1); DSR128(Bf[n][1], c1, n * 2048); DSR128(Af[n][1], a1, n * 2048); } } \
        if ((FIX) == 3) K2_FIXLD; if ((FIX) == 4) asm volatile("global_load_dword %0, %1, off sc1" : "=v"(pollv) : "v"(cnt) : "memory"); \
        if ((TY) != 2) WAIT_V(VW); else WAIT_V(0); \
        BAR; WAIT_L(0); \
        asm volatile("" : "+v"(Af[0][0]), "+v"(Af[0][1]), "+v"(Af[1][0]), "+v"(Af[1][1]), "+v"(Af[2][0]), "+v"(Af[2][1]), "+v"(Af[3][0]), "+v"(Af[3][1])); \
        if ((RT) == 0) { \
            asm volatile("" : "+v"(Bl[0][0]), "+v"(Bh[0][0]), "+v"(Bl[1][0]), "+v"(Bh[1][0]), "+v"(Bl[2][0]), "+v"(Bh[2][0]), "+v"(Bl[3][0]), "+v"(Bh[3][0]), \
                              "+v"(Bl[0][1]), "+v"(Bh[0][1]), "+v"(Bl[1][1]), "+v"(Bh[1][1]), "+v"(Bl[2][1]), "+v"(Bh[2][1]), "+v"(Bl[3][1]), "+v"(Bh[3][1])); \
            if ((FIX) == 1) K2_FIXPIN; \
            SCHED; \
            if ((FIX) == 1 || (FIX) == 2) { u32x4 ore, oim; \
                _Pragma("unroll") for (int k = 0; k < 2; ++k) _Pragma("unroll") for (int m = 0; m < 4; ++m) _Pragma("unroll") for (int n = 0; n < 4; ++n) \
                    acc[m][n] = __builtin_amdgcn_mfma_f32_16x16x32_bf16(CAT(Bl[n][k], Bh[n][k]), Af[m][k], acc[m][n], 0, 0, 0); \
                K2_FIXMATH((FIX) - 1); \
                _Pragma("unroll") for (int i = 0; i < 32; ++i) { __builtin_amdgcn_sched_group_barrier(0x8, 1, 0); __builtin_amdgcn_sched_group_barrier(0x402, 5, 0); } \
                SCHED; K2_FIXST((FIX) - 1); \
            } else K2_MMA(CAT(Bl[n][k], Bh[n][k])); \
        } else { \
            asm volatile("" : "+v"(Bf[0][0]), "+v"(Bf[0][1]), "+v"(Bf[1][0]), "+v"(Bf[1][1]), "+v"(Bf[2][0]), "+v"(Bf[2][1]), "+v"(Bf[3][0]), "+v"(Bf[3][1])); \
            SCHED; K2_MMA(Bf[n][k]); } \
        SCHED; K2_ADV; BAR; SCHED; } while (0)
#define K2_SCALE do { _Pragma("unroll") for (int m = 0; m < 4; ++m) _Pragma("unroll") for (int n = 0; n < 4; ++n) acc[m][n] *= (1.f - omg[m]); } while (0)
    unsigned pollv = 0; const unsigned* cnt = (const unsigned*)(ws + WS_CNT) + (rt >> 1) * 16;
    K2_STEPW(0, 0, 22, 3); K2_STEPW(0, 0, 8, 1); K2_STEPW(0, 0, 10, 2); K2_STEPW(0, 0, 10, 0);
    for (; sl < 11;) K2_STEP(0, 0);
    if (tid == 0) __hip_atomic_fetch_add((GAS unsigned*)(ws + WS_CNT) + fpm * 16, 1u, __ATOMIC_RELAXED, __HIP_MEMORY_SCOPE_AGENT);
    for (; sl < NF - 7;) K2_STEP(0, 0);
    K2_STEPW(0, 0, 9, 4); K2_STEP(0, 0);
    {
        asm volatile("" : "+v"(pollv));
        if ((unsigned)__builtin_amdgcn_readfirstlane(pollv) < 32u) {
            for (unsigned spins = 0; spins < (1u << 20); ++spins) {
                if ((unsigned)__builtin_amdgcn_readfirstlane(__hip_atomic_load((GAS unsigned*)cnt, __ATOMIC_RELAXED, __HIP_MEMORY_SCOPE_AGENT)) >= 32u) break;
                __builtin_amdgcn_s_sleep(2); } }
        asm volatile("" ::: "memory");
    }
    for (; sl < NF;) K2_STEP(0, 1);
    K2_SCALE;
    for (; sl < NS - 3;) K2_STEP(1, 1);
    for (; sl < NS;) K2_STEP(1, 2);
    if (grp == 0) BAR;
    {
        LAS f32x4* xs = (LAS f32x4*)(lds + ((wid & 3) * 2 + grp) * 8192);
        const LAS f32x4* xr = (const LAS f32x4*)(lds + ((wid & 3) * 2 + (grp ^ 1)) * 8192);
        f32x4 r[2][4];
        if (grp) {
#pragma unroll
            for (int mm = 0; mm < 2; ++mm)
#pragma unroll
                for (int n = 0; n < 4; ++n) { xs[(mm * 4 + n) * 64 + lane] = acc[mm][n]; r[mm][n] = acc[2 + mm][n]; }
        } else {
#pragma unroll
            for (int mm = 0; mm < 2; ++mm)
#pragma unroll
                for (int n = 0; n < 4; ++n) { xs[(mm * 4 + n) * 64 + lane] = acc[2 + mm][n]; r[mm][n] = acc[mm][n]; }
        }
        const int mt = 2 * grp + kh;
        const int row = rt * 128 + wm * 64 + mt * 16 + fr;
        const float g = ((const float*)(ws + WS_G))[row];
        u32x2 xv[4], dv[4];
#pragma unroll
        for (int n = 0; n < 4; ++n) { const int col = ct * 128 + wn * 64 + n * 16 + fq * 4;
            xv[n] = *(const u32x2*)(x + (size_t)row * D_ + col); dv[n] = *(const u32x2*)(dsk + col); }
        WAIT_L(0); BAR;
#pragma unroll
        for (int mm = 0; mm < 2; ++mm)
#pragma unroll
            for (int n = 0; n < 4; ++n) r[mm][n] += xr[(mm * 4 + n) * 64 + lane];
        f32x4 mine[4], give[4];
        if (kh) {
#pragma unroll
            for (int n = 0; n < 4; ++n) { mine[n] = r[1][n]; give[n] = r[0][n]; }
        } else {
#pragma unroll
            for (int n = 0; n < 4; ++n) { mine[n] = r[0][n]; give[n] = r[1][n]; }
        }
        const int pbid = bid ^ 4;
        const auto rs = MKRS(ws + WS_XB, 256 * 32768);
#pragma unroll
        for (int n = 0; n < 4; ++n)
            BST(__builtin_bit_cast(u32x4, give[n]), rs, bid * 32768 + wid * 4096 + n * 1024 + lane * 16, 0, 16);
#pragma unroll
        for (int n = 0; n < 4; ++n) {
            mine[n][0] += g * __uint_as_float(xv[n][0] << 16) * __uint_as_float(dv[n][0] << 16);
            mine[n][1] += g * __uint_as_float(xv[n][0] & 0xffff0000u) * __uint_as_float(dv[n][0] & 0xffff0000u);
            mine[n][2] += g * __uint_as_float(xv[n][1] << 16) * __uint_as_float(dv[n][1] << 16);
            mine[n][3] += g * __uint_as_float(xv[n][1] & 0xffff0000u) * __uint_as_float(dv[n][1] & 0xffff0000u); }
        u32x4 pv[4];
        const unsigned char* pp = ws + WS_XB + (size_t)pbid * 32768 + wid * 4096 + lane * 16;
        for (unsigned spins = 0;; ++spins) {
            asm volatile("global_load_dwordx4 %0, %4, off sc1\n\tglobal_load_dwordx4 %1, %4, off offset:1024 sc1\n\t"
                         "global_load_dwordx4 %2, %4, off offset:2048 sc1\n\tglobal_load_dwordx4 %3, %4, off offset:3072 sc1\n\ts_waitcnt vmcnt(0)"
                         : "=&v"(pv[0]), "=&v"(pv[1]), "=&v"(pv[2]), "=&v"(pv[3]) : "v"(pp) : "memory");
            bool ok = true;
#pragma unroll
            for (int n = 0; n < 4; ++n) ok &= (pv[n][0] != SENT) & (pv[n][1] != SENT) & (pv[n][2] != SENT) & (pv[n][3] != SENT);
            if (__all(ok) || spins > (1u << 18)) break;
            __builtin_amdgcn_s_sleep(1);
        }
#pragma unroll
        for (int n = 0; n < 4; ++n)
            *(f32x4*)(out + (size_t)row * D_ + ct * 128 + wn * 64 + n * 16 + fq * 4) = mine[n] + __builtin_bit_cast(f32x4, pv[n]);

    }
}
}

extern "C" void kernel_launch(void* const* d_in, const int* in_sizes, int n_in, void* d_out, int out_size, void* d_ws, size_t ws_size,
                              hipStream_t stream) {
    const u16* x = (const u16*)d_in[0]; const u16* lre = (const u16*)d_in[1]; const u16* lim = (const u16*)d_in[2];
    const u16* Bre = (const u16*)d_in[3]; const u16* Bim = (const u16*)d_in[4]; const u16* Cre = (const u16*)d_in[5]; const u16* Cim = (const u16*)d_in[6];
    const u16* ldt = (const u16*)d_in[7]; const u16* dsk = (const u16*)d_in[8];
    const u16* w1 = (const u16*)d_in[10]; const u16* w3 = (const u16*)d_in[11]; const u16* w2 = (const u16*)d_in[12]; const u16* gw = (const u16*)d_in[13];
    unsigned char* ws = (unsigned char*)d_ws;
    (void)hipFuncSetAttribute((const void*)k1_gemm, hipFuncAttributeMaxDynamicSharedMemorySize, K1_LDS);
    (void)hipFuncSetAttribute((const void*)k2_gemm, hipFuncAttributeMaxDynamicSharedMemorySize, K2_LDS);
    k1_gemm<<<256, 512, K1_LDS, stream>>>(x, w1, w3, Bre, Bim, w2, Cre, Cim, gw, lre, lim, ldt, ws);
    k2_gemm<<<256, 512, K2_LDS, stream>>>(x, w2, Cre, Cim, dsk, ws, (float*)d_out);
}
```
